```python
import torch

def _softmax(x, axis=-1):
    return torch.softmax(x, dim=axis)
def _gelu(x):
    return torch.nn.functional.gelu(x, approximate="tanh")
def _silu(x):
    return torch.nn.functional.silu(x)
def _sigmoid(x):
    return torch.sigmoid(x)
def _layernorm(x, axis=-1, eps=1e-5):
    m = x.mean(dim=axis, keepdim=True)
    v = x.var(dim=axis, keepdim=True, unbiased=False)
    return (x - m) / torch.sqrt(v + eps)
def _scatter_add(tgt, idx, src, axis=0):
    return tgt.clone().index_add_(axis, idx, src)
def _segment_sum(src, seg, n):
    out = torch.zeros((n,) + src.shape[1:], dtype=src.dtype)
    return out.index_add_(0, seg, src)
def _pad(x, pw):
    flat = tuple(v for pair in reversed(pw) for v in pair)
    return torch.nn.functional.pad(x, flat)

kernel_name = 'interleave_layers__grouped_kv_mqa__dense_activation_gelu__prompt'

def setup_inputs():

    _w = (lambda a: (lambda t: t.to(torch.bfloat16) if t.is_floating_point() else t)(torch.as_tensor(a)))
    def _A_setup():
        B, S, D, H = 1, 2048, 448, 16
        Dh = D // H
        return {
            "x": _w((lambda *s: torch.randn(*s, dtype=torch.bfloat16) * 0.05)(B, S, D)),
            "wq": _w((lambda *s: torch.randn(*s, dtype=torch.bfloat16) * 0.05)(D, H * Dh)),
            "wk": _w((lambda *s: torch.randn(*s, dtype=torch.bfloat16) * 0.05)(D, Dh)),
            "wv": _w((lambda *s: torch.randn(*s, dtype=torch.bfloat16) * 0.05)(D, Dh)),
            "wo": _w((lambda *s: torch.randn(*s, dtype=torch.bfloat16) * 0.05)(H * Dh, D)),
        }
    _A_in = _A_setup()

    def _B_setup():
        B, S, D, F = 1, 2048, 448, 1792
        return {
            "x": _w((lambda *s: torch.randn(*s, dtype=torch.bfloat16) * 0.05)(B, S, D)),
            "w1": _w((lambda *s: torch.randn(*s, dtype=torch.bfloat16) * 0.8)(D, F) * (D ** -0.5)),
            "b1": _w((lambda *s: torch.randn(*s, dtype=torch.bfloat16) * 12.8)(F) * 0.01),
            "w2": _w((lambda *s: torch.randn(*s, dtype=torch.bfloat16) * 0.2)(F, D) * (F ** -0.5)),
            "b2": _w((lambda *s: torch.randn(*s, dtype=torch.bfloat16) * 1.6)(D) * 0.01),
        }
    _B_in = _B_setup()

    return {
        "A_x": _A_in["x"],
        "A_wq": _A_in["wq"],
        "A_wk": _A_in["wk"],
        "A_wv": _A_in["wv"],
        "A_wo": _A_in["wo"],
        "B_x": _B_in["x"],
        "B_w1": _B_in["w1"],
        "B_b1": _B_in["b1"],
        "B_w2": _B_in["w2"],
        "B_b2": _B_in["b2"],
    }

def reference(A_x, A_wq, A_wk, A_wv, A_wo, B_x, B_w1, B_b1, B_w2, B_b2):
    def _A_ref(x, wq, wk, wv, wo):
        B, S, D = x.shape
        H = 16
        Dh = D // H
        q = (lambda x, *d: x.permute(*d))((x @ wq).reshape(B, S, H, Dh), 0, 2, 1, 3)
        k = x @ wk
        v = x @ wv
        scores = torch.einsum("bhqd,bkd->bhqk", q, k) * (Dh ** -0.5)
        p = _softmax(scores, axis=-1)
        o = torch.einsum("bhqk,bkd->bhqd", p, v)
        return (lambda x, *d: x.permute(*d))(o, 0, 2, 1, 3).reshape(B, S, H * Dh) @ wo

    def _B_ref(x, w1, b1, w2, b2):
        h = torch.einsum("bsd,df->bsf", x, w1) + b1
        h = _gelu(h)
        return torch.einsum("bsf,fd->bsd", h, w2) + b2

    x = A_x
    x = x + _A_ref(_layernorm(x, axis=-1), A_wq, A_wk, A_wv, A_wo)
    x = x + _B_ref(_layernorm(x, axis=-1), B_w1, B_b1, B_w2, B_b2)
    x = x + _A_ref(_layernorm(x, axis=-1), A_wq, A_wk, A_wv, A_wo)
    x = x + _B_ref(_layernorm(x, axis=-1), B_w1, B_b1, B_w2, B_b2)
    return x
```

```cpp
#include <hip/hip_runtime.h>
#include <hip/hip_bf16.h>

typedef __attribute__((ext_vector_type(16))) float f32x16;
typedef __attribute__((ext_vector_type(8))) float f32x8;
typedef __attribute__((ext_vector_type(4))) float f32x4;
typedef __attribute__((ext_vector_type(2))) float f32x2;
typedef __attribute__((ext_vector_type(8))) __bf16 bf16x8;
typedef __attribute__((ext_vector_type(16))) __bf16 bf16x16;
typedef __attribute__((ext_vector_type(2))) __bf16 bf16x2;
typedef __attribute__((ext_vector_type(4))) unsigned v4u;
typedef __attribute__((ext_vector_type(2))) unsigned v2u;
typedef unsigned short u16;
#define LAS __attribute__((address_space(3)))
#define GAS __attribute__((address_space(1)))
#define SBAR() __builtin_amdgcn_sched_barrier(0)
#define PIN(x) asm volatile("" : "+v"(x))
#define DEVINL __device__ __forceinline__

constexpr int SEQ = 2048, DM = 448, NH = 16, DH = 28, FF = 1792;
constexpr int WS_CTRL = 0;
constexpr int WS_WQ = 65536;
constexpr int WS_WK = WS_WQ + 448 * 1024;
constexpr int WS_WV = WS_WK + 28 * 1024;
constexpr int WS_WO = WS_WV + 28 * 1024;
constexpr int WS_W1 = WS_WO + 448 * 1024;
constexpr int WS_W2 = WS_W1 + 1568 * 1024;
constexpr int WS_KV = WS_W2 + 1568 * 1024;
constexpr int WS_OP = WS_KV + 2 * 256 * 1024;
constexpr int PART_BYTES = 64 * 4 * 28 * 1024;
constexpr int WS_FP = WS_OP + 2 * PART_BYTES;
constexpr int WS_X1 = WS_FP + 2 * PART_BYTES;
constexpr int WS_X2 = WS_X1 + 64 * 28 * 2048;
constexpr int WS_END = WS_X2 + 64 * 28 * 2048;
constexpr int L_XN = 0;
constexpr int L_H = 28672;
constexpr int L_OI = 57344;
constexpr int L_Q = 65536;
constexpr int L_ST = 73728;
constexpr int L_RED = 0;
constexpr int L_BIG = 77824;
constexpr int LDS_BYTES = L_BIG + 65536 + 16384;

struct Params {
  const u16 *x, *wq, *wk, *wv, *wo, *w1, *b1, *w2, *b2;
  u16* out;
  unsigned char* ws;
};

DEVINL f32x16 mfma32(v4u a, v4u b, f32x16 c) { return __builtin_amdgcn_mfma_f32_32x32x16_bf16(__builtin_bit_cast(bf16x8, a), __builtin_bit_cast(bf16x8, b), c, 0, 0, 0); }
DEVINL f32x4 mfma16(v4u a, v4u b, f32x4 c) { return __builtin_amdgcn_mfma_f32_16x16x32_bf16(__builtin_bit_cast(bf16x8, a), __builtin_bit_cast(bf16x8, b), c, 0, 0, 0); }
DEVINL f32x16 smf(v4u a, f32x8 b, f32x16 c, int idx) { return __builtin_amdgcn_smfmac_f32_32x32x32_bf16(__builtin_bit_cast(bf16x8, a), __builtin_bit_cast(bf16x16, b), c, idx, 0, 0); }
DEVINL unsigned pk2(float lo, float hi) { f32x2 v = {lo, hi}; return __builtin_bit_cast(unsigned, __builtin_convertvector(v, bf16x2)); }
DEVINL float bflo(unsigned u) { return __builtin_bit_cast(float, u << 16); }
DEVINL float bfhi(unsigned u) { return __builtin_bit_cast(float, u & 0xffff0000u); }
DEVINL float swap32_add(float v) { auto r = __builtin_amdgcn_permlane32_swap(__builtin_bit_cast(unsigned, v), __builtin_bit_cast(unsigned, v), false, false); return __uint_as_float(r[0]) + __uint_as_float(r[1]); }
DEVINL float swap16_add(float v) { auto r = __builtin_amdgcn_permlane16_swap(__builtin_bit_cast(unsigned, v), __builtin_bit_cast(unsigned, v), false, false); return __uint_as_float(r[0]) + __uint_as_float(r[1]); }
DEVINL float swap32_max(float v) { auto r = __builtin_amdgcn_permlane32_swap(__builtin_bit_cast(unsigned, v), __builtin_bit_cast(unsigned, v), false, false); return fmaxf(__uint_as_float(r[0]), __uint_as_float(r[1])); }
DEVINL float red_g(float v) { return swap32_add(swap16_add(v)); }
DEVINL float wave_max(float v) {
#pragma unroll
  for (int o = 1; o < 64; o <<= 1) v = fmaxf(v, __shfl_xor(v, o));
  return v;
}
typedef __amdgpu_buffer_rsrc_t rsrc_t;
DEVINL rsrc_t mkrs(const void* p, int bytes) { return __builtin_amdgcn_make_buffer_rsrc((void*)p, 0, bytes, 0x00020000); }
template <int AUX> DEVINL v4u ldf(rsrc_t rs, int voff, int soff) { return __builtin_amdgcn_raw_buffer_load_b128(rs, voff, soff, AUX); }
template <int AUX> DEVINL void stf(v4u v, rsrc_t rs, int voff, int soff) { __builtin_amdgcn_raw_buffer_store_b128(v, rs, voff, soff, AUX); }
DEVINL v4u lds128(LAS unsigned char* L, int off) { return *(LAS v4u*)(L + off); }

__host__ __device__ constexpr int perm16(int kg, int j) { return 32 * (kg >> 2) + 16 * (j >> 2) + 4 * (kg & 3) + (j & 3); }

template <int NT> DEVINL void layer_norm(const f32x4 (&x)[NT][2], LAS unsigned char* L, int w, int t0, int lane) {
  const int n = lane & 15, g = lane >> 4;
  LAS f32x2* st = (LAS f32x2*)(L + L_ST);
#pragma unroll
  for (int tt = 0; tt < 2; ++tt) {
    float s = 0.f;
#pragma unroll
    for (int i = 0; i < NT; ++i) s += (x[i][tt][0] + x[i][tt][1]) + (x[i][tt][2] + x[i][tt][3]);
    s = red_g(s);
    const float m = s * (1.0f / (16 * NT));
    float q = 0.f;
#pragma unroll
    for (int i = 0; i < NT; ++i)
#pragma unroll
      for (int j = 0; j < 4; ++j) { const float d = x[i][tt][j] - m; q += d * d; }
    q = red_g(q);
    if (g == 0) st[(16 * tt + n) * 8 + w] = f32x2{m, q};
  }
  __syncthreads();
#pragma unroll
  for (int tt = 0; tt < 2; ++tt) {
    const f32x4 a = *(LAS f32x4*)(L + L_ST + ((16 * tt + n) * 8 + 2 * g) * 8);
    const float nw = g < 2 ? 64.f : 48.f;
    const float mu = red_g((a[0] + a[2]) * nw) * (1.0f / DM);
    const float d0 = a[0] - mu, d1 = a[2] - mu;
    const float m2 = red_g(a[1] + a[3] + nw * (d0 * d0 + d1 * d1));
    const float rs = __builtin_amdgcn_rsqf(m2 * (1.0f / DM) + 1e-5f);
#pragma unroll
    for (int i = 0; i < NT; ++i) {
      const int ft = t0 + i;
      v2u o; o[0] = pk2((x[i][tt][0] - mu) * rs, (x[i][tt][1] - mu) * rs); o[1] = pk2((x[i][tt][2] - mu) * rs, (x[i][tt][3] - mu) * rs);
      *(LAS v2u*)(L + L_XN + (ft >> 1) * 2048 + g * 512 + tt * 256 + n * 16 + (ft & 1) * 8) = o;
    }
  }
  __syncthreads();
}

template <int NT> DEVINL void load_x_rows(f32x4 (&x)[NT][2], const u16* xg, int grp, int t0, int lane) {
  const int n = lane & 15, g = lane >> 4;
#pragma unroll
  for (int i = 0; i < NT; ++i)
#pragma unroll
    for (int tt = 0; tt < 2; ++tt) {
      const v2u v = *(const GAS v2u*)(xg + (32 * grp + 16 * tt + n) * DM + 16 * (t0 + i) + 4 * g);
      x[i][tt] = f32x4{bflo(v[0]), bfhi(v[0]), bflo(v[1]), bfhi(v[1])};
    }
}
template <int NT> DEVINL void load_x_acc(f32x4 (&x)[NT][2], rsrc_t rs, int base, int grp, int t0, int lane) {
#pragma unroll
  for (int i = 0; i < NT; ++i)
#pragma unroll
    for (int tt = 0; tt < 2; ++tt) x[i][tt] = __builtin_bit_cast(f32x4, ldf<0>(rs, lane * 16, base + ((grp * 28 + t0 + i) * 2 + tt) * 1024));
}
template <int NT> DEVINL void store_x_acc(const f32x4 (&x)[NT][2], rsrc_t rs, int base, int grp, int t0, int lane) {
#pragma unroll
  for (int i = 0; i < NT; ++i)
#pragma unroll
    for (int tt = 0; tt < 2; ++tt) stf<0>(__builtin_bit_cast(v4u, x[i][tt]), rs, lane * 16, base + ((grp * 28 + t0 + i) * 2 + tt) * 1024);
}
template <int NT, int AUX> DEVINL void add_partials(f32x4 (&x)[NT][2], rsrc_t rs, int base, int grp, int t0, int lane) {
  v4u p[4][NT];
#pragma unroll
  for (int c = 0; c < 4; ++c)
#pragma unroll
    for (int i = 0; i < NT; ++i) p[c][i] = ldf<AUX>(rs, lane * 16, base + ((grp * 4 + c) * 28 + t0 + i) * 1024);
#pragma unroll
  for (int i = 0; i < NT; ++i)
#pragma unroll
    for (int tt = 0; tt < 2; ++tt) {
      f32x4 s = {0.f, 0.f, 0.f, 0.f};
#pragma unroll
      for (int c = 0; c < 4; ++c) s += f32x4{bflo(p[c][i][2 * tt]), bfhi(p[c][i][2 * tt]), bflo(p[c][i][2 * tt + 1]), bfhi(p[c][i][2 * tt + 1])};
      x[i][tt] += s;
    }
}
template <int NT, int AUX> DEVINL void store_partial(const f32x4 (&a)[NT][2], rsrc_t rs, int base, int grp, int c, int t0, int lane) {
#pragma unroll
  for (int i = 0; i < NT; ++i) {
    v4u o = {pk2(a[i][0][0], a[i][0][1]), pk2(a[i][0][2], a[i][0][3]), pk2(a[i][1][0], a[i][1][1]), pk2(a[i][1][2], a[i][1][3])};
    stf<AUX>(o, rs, lane * 16, base + ((grp * 4 + c) * 28 + t0 + i) * 1024);
  }
}
template <int NT> DEVINL void add_bias(f32x4 (&x)[NT][2], const u16* b, int t0, int lane) {
  const int g = lane >> 4;
#pragma unroll
  for (int i = 0; i < NT; ++i) {
    const v2u v = *(const GAS v2u*)(b + 16 * (t0 + i) + 4 * g);
    const f32x4 bb = {bflo(v[0]), bfhi(v[0]), bflo(v[1]), bfhi(v[1])};
    x[i][0] += bb; x[i][1] += bb;
  }
}

template <int AUX> DEVINL void kv_proj(const v4u (&wf)[7], LAS unsigned char* L, rsrc_t rs, unsigned char* ws, int layer, int grp, int w, int lane) {
  const int isv = w >> 2, qd = w & 3, hh = lane >> 5, r = lane & 31;
  const int rp = isv ? ((r & 19) | ((r & 4) << 1) | ((r & 8) >> 1)) : r;
  f32x16 acc = {};
#pragma unroll
  for (int s = 0; s < 7; ++s) {
    const v4u xf = lds128(L, L_XN + (7 * qd + s) * 1024 + (hh * 32 + rp) * 16);
    acc = isv ? mfma32(xf, wf[s], acc) : mfma32(wf[s], xf, acc);
  }
  if (qd) {
#pragma unroll
    for (int k = 0; k < 4; ++k) *(LAS f32x4*)(L + L_BIG + ((isv * 3 + qd - 1) * 4 + k) * 1024 + lane * 16) = f32x4{acc[4 * k], acc[4 * k + 1], acc[4 * k + 2], acc[4 * k + 3]};
  }
  __syncthreads();
  if (!qd) {
#pragma unroll
    for (int q = 0; q < 3; ++q)
#pragma unroll
      for (int k = 0; k < 4; ++k) {
        const f32x4 v = *(LAS f32x4*)(L + L_BIG + ((isv * 3 + q) * 4 + k) * 1024 + lane * 16);
        acc[4 * k] += v[0]; acc[4 * k + 1] += v[1]; acc[4 * k + 2] += v[2]; acc[4 * k + 3] += v[3];
      }
    v4u f0, f1;
#pragma unroll
    for (int k = 0; k < 4; ++k) { f0[k] = pk2(acc[2 * k], acc[2 * k + 1]); f1[k] = pk2(acc[8 + 2 * k], acc[9 + 2 * k]); }
    if (isv) {
      if (r >= 28) { const unsigned c = r == 28 ? 0x3f803f80u : 0u; f0 = v4u{c, c, c, c}; f1 = f0; }
    } else {
      if (hh) { f1[2] = 0u; f1[3] = 0u; }
      float q2 = 0.f;
#pragma unroll
      for (int k = 0; k < 4; ++k) { q2 += bflo(f0[k]) * bflo(f0[k]) + bfhi(f0[k]) * bfhi(f0[k]) + bflo(f1[k]) * bflo(f1[k]) + bfhi(f1[k]) * bfhi(f1[k]); }
      q2 = wave_max(swap32_add(q2));
      if (hh) { f1[2] = 0x3f803f80u; f1[3] = 0x00003f80u; }
      if (lane == 0) ((GAS float*)(ws + WS_CTRL))[layer * 64 + grp] = q2;
    }
    const int base = WS_KV + (layer * 64 + grp) * 4096 + isv * 2048;
    stf<AUX>(f0, rs, lane * 16, base);
    stf<AUX>(f1, rs, lane * 16, base + 1024);
  }
}

DEVINL v4u gather8(const u16* src, int stride, const int (&row)[8], int col, bool ok) {
  u16 e[8];
#pragma unroll
  for (int j = 0; j < 8; ++j) e[j] = (ok && row[j] >= 0) ? ((const GAS u16*)src)[row[j] * stride + col] : (u16)0;
  return v4u{e[0] | ((unsigned)e[1] << 16), e[2] | ((unsigned)e[3] << 16), e[4] | ((unsigned)e[5] << 16), e[6] | ((unsigned)e[7] << 16)};
}
DEVINL v4u frag32(const u16* src, int stride, int col0, int s, int lane) {
  const int d = lane & 31, hh = lane >> 5;
  int row[8];
#pragma unroll
  for (int j = 0; j < 8; ++j) row[j] = perm16(2 * s + hh, j);
  return gather8(src, stride, row, col0 + d, d < 28);
}
template <int NT> DEVINL void k0_kv(const Params& p, LAS unsigned char* L, rsrc_t rs, int grp, int w, int t0, int lane) {
  f32x4 x[NT][2];
  load_x_rows<NT>(x, p.x, grp, t0, lane);
  v4u wf[7];
#pragma unroll
  for (int s = 0; s < 7; ++s) wf[s] = frag32((w >> 2) ? p.wv : p.wk, DH, 0, 7 * (w & 3) + s, lane);
  layer_norm<NT>(x, L, w, t0, lane);
  kv_proj<0>(wf, L, rs, p.ws, 0, grp, w, lane);
}
__global__ void __launch_bounds__(512, 2) k0(Params p) {
  extern __shared__ __attribute__((aligned(16))) unsigned char smem[];
  LAS unsigned char* L = (LAS unsigned char*)smem;
  const int lane = threadIdx.x & 63, w = __builtin_amdgcn_readfirstlane(threadIdx.x >> 6);
  const rsrc_t rs = mkrs(p.ws, WS_END);
  const int b = blockIdx.x;
  if (b < 64) {
    if (w < 4) k0_kv<4>(p, L, rs, b, w, 4 * w, lane); else k0_kv<3>(p, L, rs, b, w, 16 + 3 * (w - 4), lane);
    return;
  }
  const int f = (b - 64) * 8 + w;
  const int n = lane & 15, g = lane >> 4;
  v4u v; int dst;
  int row[8];
  if (f < 448) { const int s = f >> 4, h = f & 15; v = frag32(p.wq, DM, DH * h, s, lane); dst = WS_WQ + (h * 28 + s) * 1024; }
  else if (f < 476) { v = frag32(p.wk, DH, 0, f - 448, lane); dst = WS_WK + (f - 448) * 1024; }
  else if (f < 504) { v = frag32(p.wv, DH, 0, f - 476, lane); dst = WS_WV + (f - 476) * 1024; }
  else if (f < 952) { const int q = f - 504, h = q / 28, ft = q % 28;
#pragma unroll
    for (int j = 0; j < 8; ++j) { const int d = 16 * (g >> 1) + 8 * (j >> 2) + 4 * (g & 1) + (j & 3); row[j] = d < 28 ? DH * h + d : -1; }
    v = gather8(p.wo, DM, row, 16 * ft + n, true); dst = WS_WO + (ft * 16 + h) * 1024; }
  else if (f < 2520) { const int q = f - 952, pp = q / 112, ht = q % 112;
#pragma unroll
    for (int j = 0; j < 8; ++j) row[j] = perm16(4 * pp + g, j);
    v = gather8(p.w1, FF, row, 16 * ht + n, true); dst = WS_W1 + (ht * 14 + pp) * 1024; }
  else if (f < 4088) { const int q = f - 2520, cp = q / 28, ft = q % 28, c = cp / 14, pp = cp % 14;
#pragma unroll
    for (int j = 0; j < 8; ++j) row[j] = 448 * c + perm16(4 * pp + g, j);
    v = gather8(p.w2, DM, row, 16 * ft + n, true); dst = WS_W2 + ((c * 28 + ft) * 14 + pp) * 1024; }
  else return;
  stf<0>(v, rs, lane * 16, dst);
}

#define EX(x) __builtin_amdgcn_exp2f(x)
constexpr float QSCALE = 0.18898223650461362f * 1.4426950408889634f;
constexpr float BOUND_MAX = 40.f;

DEVINL void q_proj(LAS unsigned char* L, rsrc_t rs, int c, int w, int lane) {
  if (w < 4) {
    v4u wq[28];
#pragma unroll
    for (int s = 0; s < 28; ++s) wq[s] = ldf<0>(rs, lane * 16, WS_WQ + ((4 * c + w) * 28 + s) * 1024);
    f32x16 acc = {};
#pragma unroll
    for (int s = 0; s < 28; ++s) acc = mfma32(wq[s], lds128(L, L_XN + s * 1024 + lane * 16), acc);
    v4u f0, f1;
#pragma unroll
    for (int k = 0; k < 4; ++k) { f0[k] = pk2(acc[2 * k] * QSCALE, acc[2 * k + 1] * QSCALE); f1[k] = pk2(acc[8 + 2 * k] * QSCALE, acc[9 + 2 * k] * QSCALE); }
    float q2 = 0.f;
#pragma unroll
    for (int k = 0; k < 4; ++k) { q2 += bflo(f0[k]) * bflo(f0[k]) + bfhi(f0[k]) * bfhi(f0[k]) + bflo(f1[k]) * bflo(f1[k]) + bfhi(f1[k]) * bfhi(f1[k]); }
    q2 = swap32_add(q2);
    *(LAS v4u*)(L + L_Q + w * 2048 + lane * 16) = f0;
    *(LAS v4u*)(L + L_Q + w * 2048 + 1024 + lane * 16) = f1;
    if (lane < 32) ((LAS float*)(L + L_ST + 2048))[w * 32 + lane] = q2;
  }
  __syncthreads();
}

template <int AUX> DEVINL void attention(LAS unsigned char* L, rsrc_t rs, unsigned char* ws, int layer, int w, int lane) {
  const int kvb = WS_KV + (layer * 64 + w * 8) * 4096;
#define LD(t, f) ldf<AUX>(rs, lane * 16 + (f) * 1024, kvb + (t) * 4096)
  v4u k0 = LD(0, 0), k1 = LD(0, 1), v0 = LD(0, 2), v1 = LD(0, 3);
  v4u k0n = LD(1, 0), k1n = LD(1, 1), v0n = LD(1, 2), v1n = LD(1, 3);
  const float km2 = wave_max(((const GAS float*)(ws + WS_CTRL))[layer * 64 + lane]);
  v4u Q0[4], Q1[4];
  float cb[4];
  float cmax = 0.f;
#pragma unroll
  for (int h = 0; h < 4; ++h) {
    Q0[h] = lds128(L, L_Q + h * 2048 + lane * 16); Q1[h] = lds128(L, L_Q + h * 2048 + 1024 + lane * 16);
    cb[h] = __builtin_sqrtf(((LAS float*)(L + L_ST + 2048))[h * 32 + (lane & 31)] * km2) * 1.01f;
    cmax = fmaxf(cmax, cb[h]);
  }
  const f32x16 zero = {};
  if (!(wave_max(cmax) <= BOUND_MAX)) {
    LAS float* mx = (LAS float*)(L + L_BIG);
#pragma unroll
    for (int h = 0; h < 4; ++h) {
      float m = -INFINITY;
#pragma unroll 1
      for (int t = 0; t < 8; ++t) {
        f32x16 s = mfma32(LD(t, 0), Q0[h], zero); s = mfma32(LD(t, 1), Q1[h], s);
#pragma unroll
        for (int r = 0; r < 16; ++r) m = fmaxf(m, s[r]);
      }
      m = swap32_max(m);
      if (lane < 32) mx[(h * 8 + w) * 32 + lane] = m;
    }
    __syncthreads();
#pragma unroll
    for (int h = 0; h < 4; ++h) {
      float m = -INFINITY;
#pragma unroll
      for (int ww = 0; ww < 8; ++ww) m = fmaxf(m, mx[(h * 8 + ww) * 32 + (lane & 31)]);
      cb[h] = m;
    }
    __syncthreads();
  }
#pragma unroll
  for (int h = 0; h < 4; ++h) {
    const float c = -cb[h];
    const unsigned c0 = pk2(c, 0.f) & 0xffffu; const float r1 = c - bflo(c0);
    const unsigned c1 = pk2(r1, 0.f) & 0xffffu; const float r2 = r1 - bflo(c1);
    const unsigned c2 = pk2(r2, 0.f) & 0xffffu;
    if (lane >= 32) { Q1[h][2] = c0 | (c1 << 16); Q1[h][3] = c2; }
  }
  int idx = 0xdddd; PIN(idx);
  f32x16 O[4] = {};
  v4u k0m, k1m, v0m, v1m, v1p = {0, 0, 0, 0};
  f32x16 S, Sn; f32x8 Phi = {};
  S = mfma32(k0, Q0[0], zero); S = mfma32(k1, Q1[0], S);
#pragma unroll
  for (int t = 0; t < 8; ++t) {
#pragma unroll
    for (int h = 0; h < 4; ++h) {
      f32x8 Plo, Ph2;
      const bool more = !(t == 7 && h == 3);
      SBAR();
      if (more) Sn = mfma32(h == 3 ? k0n : k0, Q0[(h + 1) & 3], zero);
      SBAR();
      if (t + 2 < 8) { if (h == 0) k0m = LD(t + 2, 0); if (h == 1) k1m = LD(t + 2, 1); if (h == 2) v0m = LD(t + 2, 2); if (h == 3) v1m = LD(t + 2, 3); }
      SBAR();
      Plo[0] = EX(S[0]); Plo[1] = EX(S[1]); Plo[2] = EX(S[2]); Plo[3] = EX(S[3]); SBAR();
      if (more) Sn = mfma32(h == 3 ? k1n : k1, Q1[(h + 1) & 3], Sn);
      SBAR();
      Plo[4] = EX(S[4]); Plo[5] = EX(S[5]); Plo[6] = EX(S[6]); Plo[7] = EX(S[7]); SBAR();
      if (t || h) O[(h + 3) & 3] = smf(h == 0 ? v1p : v1, Phi, O[(h + 3) & 3], idx);
      SBAR();
      Ph2[0] = EX(S[8]); Ph2[1] = EX(S[9]); Ph2[2] = EX(S[10]); Ph2[3] = EX(S[11]); SBAR();
      O[h] = smf(v0, Plo, O[h], idx); SBAR();
      Ph2[4] = EX(S[12]); Ph2[5] = EX(S[13]); Ph2[6] = EX(S[14]); Ph2[7] = EX(S[15]); PIN(Ph2); SBAR();
      Phi = Ph2; S = Sn;
    }
    v1p = v1; k0 = k0n; k1 = k1n; v0 = v0n; v1 = v1n;
    k0n = k0m; k1n = k1m; v0n = v0m; v1n = v1m;
  }
  O[3] = smf(v1p, Phi, O[3], idx);
#undef LD
  LAS unsigned char* mine = L + (w < 3 ? w * 16384 : L_BIG + (w - 3) * 16384);
#pragma unroll
  for (int h = 0; h < 4; ++h) {
#pragma unroll
    for (int k = 0; k < 4; ++k) *(LAS f32x4*)(mine + (h * 4 + k) * 1024 + lane * 16) = f32x4{O[h][4 * k], O[h][4 * k + 1], O[h][4 * k + 2], O[h][4 * k + 3]};
  }
  __syncthreads();
  {
    f32x4 a0 = {0.f, 0.f, 0.f, 0.f}, a1 = a0; float l = 0.f;
    const int h = w >> 1;
#pragma unroll
    for (int ww = 0; ww < 8; ++ww) {
      LAS unsigned char* src = L + (ww < 3 ? ww * 16384 : L_BIG + (ww - 3) * 16384);
      a0 += *(LAS f32x4*)(src + (2 * w) * 1024 + lane * 16);
      a1 += *(LAS f32x4*)(src + (2 * w + 1) * 1024 + lane * 16);
      l += *(LAS float*)(src + (h * 4 + 3) * 1024 + (32 + (lane & 31)) * 16);
    }
    const float il = 1.0f / l;
    v4u o = {pk2(a0[0] * il, a0[1] * il), pk2(a0[2] * il, a0[3] * il), pk2(a1[0] * il, a1[1] * il), pk2(a1[2] * il, a1[3] * il)};
    *(LAS v4u*)(L + L_OI + w * 1024 + lane * 16) = o;
  }
  __syncthreads();
}

template <int NT> DEVINL void o_proj(f32x4 (&acc)[NT][2], LAS unsigned char* L, rsrc_t rs, int c, int t0, int lane) {
  const int n = lane & 15, g = lane >> 4;
  v4u wo[NT][4];
#pragma unroll
  for (int i = 0; i < NT; ++i)
#pragma unroll
    for (int h = 0; h < 4; ++h) wo[i][h] = ldf<0>(rs, lane * 16, WS_WO + ((t0 + i) * 16 + 4 * c + h) * 1024);
#pragma unroll
  for (int i = 0; i < NT; ++i) { acc[i][0] = f32x4{0.f, 0.f, 0.f, 0.f}; acc[i][1] = acc[i][0]; }
#pragma unroll
  for (int h = 0; h < 4; ++h) {
    const v4u b0 = lds128(L, L_OI + h * 2048 + g * 512 + n * 16), b1 = lds128(L, L_OI + h * 2048 + g * 512 + 256 + n * 16);
#pragma unroll
    for (int i = 0; i < NT; ++i) { acc[i][0] = mfma16(wo[i][h], b0, acc[i][0]); acc[i][1] = mfma16(wo[i][h], b1, acc[i][1]); }
  }
}

template <int NT> DEVINL void ka_body(const Params& p, LAS unsigned char* L, rsrc_t rs, int layer, int grp, int c, int w, int t0, int lane) {
  {
    f32x4 x[NT][2];
    if (layer == 0) load_x_rows<NT>(x, p.x, grp, t0, lane); else load_x_acc<NT>(x, rs, WS_X2, grp, t0, lane);
    layer_norm<NT>(x, L, w, t0, lane);
  }
}
template <int NT> DEVINL void ka_tail(LAS unsigned char* L, rsrc_t rs, int layer, int grp, int c, int t0, int lane) {
  f32x4 acc[NT][2];
  o_proj<NT>(acc, L, rs, c, t0, lane);
  store_partial<NT, 0>(acc, rs, WS_OP + layer * PART_BYTES, grp, c, t0, lane);
}
__global__ void __launch_bounds__(512, 2) ka(Params p, int layer) {
  extern __shared__ __attribute__((aligned(16))) unsigned char smem[];
  LAS unsigned char* L = (LAS unsigned char*)smem;
  const int lane = threadIdx.x & 63, w = __builtin_amdgcn_readfirstlane(threadIdx.x >> 6);
  const rsrc_t rs = mkrs(p.ws, WS_END);
  const int b = blockIdx.x, grp = (b & 7) * 8 + (b >> 5), c = (b >> 3) & 3;
  if (w < 4) ka_body<4>(p, L, rs, layer, grp, c, w, 4 * w, lane); else ka_body<3>(p, L, rs, layer, grp, c, w, 16 + 3 * (w - 4), lane);
  q_proj(L, rs, c, w, lane);
  attention<0>(L, rs, p.ws, layer, w, lane);
  if (w < 4) ka_tail<4>(L, rs, layer, grp, c, 4 * w, lane); else ka_tail<3>(L, rs, layer, grp, c, 16 + 3 * (w - 4), lane);
}

DEVINL float gelu(float x) {
  constexpr float k0 = -2.0f * 0.7978845608028654f * 1.4426950408889634f, k1 = k0 * 0.044715f;
  const float z = x * (k0 + k1 * x * x);
  return x * __builtin_amdgcn_rcpf(1.0f + __builtin_amdgcn_exp2f(z));
}
template <int NT, int WIN> DEVINL void gemm448(f32x4 (&acc)[NT][2], LAS unsigned char* L, int img, rsrc_t rs, int wbase, int lane) {
  const int n = lane & 15, g = lane >> 4;
  constexpr int NF = 14 * NT;
  v4u a[WIN];
#pragma unroll
  for (int f = 0; f < WIN; ++f) a[f] = ldf<0>(rs, lane * 16, wbase + ((f % NT) * 14 + f / NT) * 1024);
  SBAR();
#pragma unroll
  for (int pp = 0; pp < 14; ++pp) {
    const v4u b0 = lds128(L, img + pp * 2048 + g * 512 + n * 16), b1 = lds128(L, img + pp * 2048 + g * 512 + 256 + n * 16);
#pragma unroll
    for (int i = 0; i < NT; ++i) {
      const int f = pp * NT + i;
      const v4u cur = a[f % WIN];
      if (f + WIN < NF) a[f % WIN] = ldf<0>(rs, lane * 16, wbase + (((f + WIN) % NT) * 14 + (f + WIN) / NT) * 1024);
      acc[i][0] = mfma16(cur, b0, acc[i][0]); acc[i][1] = mfma16(cur, b1, acc[i][1]);
    }
    SBAR();
  }
}
template <int NT> DEVINL void kf_body(const Params& p, LAS unsigned char* L, rsrc_t rs, int layer, int grp, int c, int w, int t0, int lane) {
  const int n = lane & 15, g = lane >> 4;
  {
    f32x4 x[NT][2];
    if (layer == 0) load_x_rows<NT>(x, p.x, grp, t0, lane); else load_x_acc<NT>(x, rs, WS_X2, grp, t0, lane);
    add_partials<NT, 0>(x, rs, WS_OP + layer * PART_BYTES, grp, t0, lane);
    if (c == 0) store_x_acc<NT>(x, rs, WS_X1, grp, t0, lane);
    layer_norm<NT>(x, L, w, t0, lane);
  }
  f32x4 acc[NT][2];
#pragma unroll
  for (int i = 0; i < NT; ++i) { acc[i][0] = f32x4{0.f, 0.f, 0.f, 0.f}; acc[i][1] = acc[i][0]; }
  add_bias<NT>(acc, p.b1 + 448 * c, t0, lane);
  gemm448<NT, 32>(acc, L, L_XN, rs, WS_W1 + (28 * c + t0) * 14 * 1024, lane);
#pragma unroll
  for (int i = 0; i < NT; ++i)
#pragma unroll
    for (int tt = 0; tt < 2; ++tt) {
      const int ft = t0 + i;
      v2u o; o[0] = pk2(gelu(acc[i][tt][0]), gelu(acc[i][tt][1])); o[1] = pk2(gelu(acc[i][tt][2]), gelu(acc[i][tt][3]));
      *(LAS v2u*)(L + L_H + (ft >> 1) * 2048 + g * 512 + tt * 256 + n * 16 + (ft & 1) * 8) = o;
    }
  __syncthreads();
#pragma unroll
  for (int i = 0; i < NT; ++i) { acc[i][0] = f32x4{0.f, 0.f, 0.f, 0.f}; acc[i][1] = acc[i][0]; }
  gemm448<NT, 32>(acc, L, L_H, rs, WS_W2 + (28 * c + t0) * 14 * 1024, lane);
  store_partial<NT, 0>(acc, rs, WS_FP + layer * PART_BYTES, grp, c, t0, lane);
}
__global__ void __launch_bounds__(512, 2) kf(Params p, int layer) {
  extern __shared__ __attribute__((aligned(16))) unsigned char smem[];
  LAS unsigned char* L = (LAS unsigned char*)smem;
  const int lane = threadIdx.x & 63, w = __builtin_amdgcn_readfirstlane(threadIdx.x >> 6);
  const rsrc_t rs = mkrs(p.ws, WS_END);
  const int b = blockIdx.x, grp = (b & 7) * 8 + (b >> 5), c = (b >> 3) & 3;
  if (w < 4) kf_body<4>(p, L, rs, layer, grp, c, w, 4 * w, lane); else kf_body<3>(p, L, rs, layer, grp, c, w, 16 + 3 * (w - 4), lane);
}

template <int NT> DEVINL void kr_body(const Params& p, LAS unsigned char* L, rsrc_t rs, int layer, int grp, int w, int t0, int lane) {
  const int n = lane & 15, g = lane >> 4;
  f32x4 x[NT][2];
  load_x_acc<NT>(x, rs, WS_X1, grp, t0, lane);
  add_partials<NT, 0>(x, rs, WS_FP + layer * PART_BYTES, grp, t0, lane);
  add_bias<NT>(x, p.b2, t0, lane);
  if (layer == 1) {
#pragma unroll
    for (int i = 0; i < NT; ++i)
#pragma unroll
      for (int tt = 0; tt < 2; ++tt) {
        v2u o; o[0] = pk2(x[i][tt][0], x[i][tt][1]); o[1] = pk2(x[i][tt][2], x[i][tt][3]);
        *(GAS v2u*)(p.out + (32 * grp + 16 * tt + n) * DM + 16 * (t0 + i) + 4 * g) = o;
      }
    return;
  }
  store_x_acc<NT>(x, rs, WS_X2, grp, t0, lane);
  v4u wf[7];
#pragma unroll
  for (int s = 0; s < 7; ++s) wf[s] = ldf<0>(rs, lane * 16, WS_WK + (7 * w + s) * 1024);
  layer_norm<NT>(x, L, w, t0, lane);
  kv_proj<0>(wf, L, rs, p.ws, 1, grp, w, lane);
}
__global__ void __launch_bounds__(512, 2) kr(Params p, int layer) {
  extern __shared__ __attribute__((aligned(16))) unsigned char smem[];
  LAS unsigned char* L = (LAS unsigned char*)smem;
  const int lane = threadIdx.x & 63, w = __builtin_amdgcn_readfirstlane(threadIdx.x >> 6);
  const rsrc_t rs = mkrs(p.ws, WS_END);
  const int b = blockIdx.x, grp = (b & 7) * 8 + (b >> 3);
  if (w < 4) kr_body<4>(p, L, rs, layer, grp, w, 4 * w, lane); else kr_body<3>(p, L, rs, layer, grp, w, 16 + 3 * (w - 4), lane);
}

extern "C" void kernel_launch(void* const* d_in, const int* in_sizes, int n_in, void* d_out, int out_size, void* d_ws, size_t ws_size, hipStream_t stream) {
  Params p;
  p.x = (const u16*)d_in[0]; p.wq = (const u16*)d_in[1]; p.wk = (const u16*)d_in[2]; p.wv = (const u16*)d_in[3]; p.wo = (const u16*)d_in[4];
  p.w1 = (const u16*)d_in[6]; p.b1 = (const u16*)d_in[7]; p.w2 = (const u16*)d_in[8]; p.b2 = (const u16*)d_in[9];
  p.out = (u16*)d_out; p.ws = (unsigned char*)d_ws;
  (void)hipFuncSetAttribute((const void*)k0, hipFuncAttributeMaxDynamicSharedMemorySize, LDS_BYTES);
  (void)hipFuncSetAttribute((const void*)ka, hipFuncAttributeMaxDynamicSharedMemorySize, LDS_BYTES);
  (void)hipFuncSetAttribute((const void*)kf, hipFuncAttributeMaxDynamicSharedMemorySize, LDS_BYTES);
  (void)hipFuncSetAttribute((const void*)kr, hipFuncAttributeMaxDynamicSharedMemorySize, LDS_BYTES);
  k0<<<64 + 511, 512, L_BIG + 24576, stream>>>(p);
  for (int layer = 0; layer < 2; ++layer) {
    ka<<<256, 512, LDS_BYTES, stream>>>(p, layer);
    kf<<<256, 512, L_BIG, stream>>>(p, layer);
    kr<<<64, 512, L_BIG + 24576, stream>>>(p, layer);
  }
}
```

```cpp
#define CODEWARM 64
#define WWARM 0
#include <hip/hip_runtime.h>
#include <hip/hip_bf16.h>

typedef __attribute__((ext_vector_type(16))) float f32x16;
typedef __attribute__((ext_vector_type(8))) float f32x8;
typedef __attribute__((ext_vector_type(4))) float f32x4;
typedef __attribute__((ext_vector_type(2))) float f32x2;
typedef __attribute__((ext_vector_type(8))) __bf16 bf16x8;
typedef __attribute__((ext_vector_type(16))) __bf16 bf16x16;
typedef __attribute__((ext_vector_type(2))) __bf16 bf16x2;
typedef __attribute__((ext_vector_type(4))) short s16x4;
typedef __attribute__((ext_vector_type(4))) unsigned v4u;
typedef __attribute__((ext_vector_type(2))) unsigned v2u;
typedef unsigned short u16;
#define LAS __attribute__((address_space(3)))
#define GAS __attribute__((address_space(1)))
#define SBAR() __builtin_amdgcn_sched_barrier(0)
#define PIN(x) asm volatile("" : "+v"(x))
#define DEVINL __device__ __forceinline__
#define BARRIER() asm volatile("s_waitcnt lgkmcnt(0)\n\ts_barrier" ::: "memory")
#define DRAIN() asm volatile("s_waitcnt vmcnt(0)" ::: "memory")
#define UNROLL _Pragma("unroll")
constexpr int SC1 = 16;

constexpr int DM = 448, DH = 28, FF = 1792;
constexpr int WS_EPOCH = 0;
constexpr int WS_KVF = 4096;
constexpr int WS_PF = WS_KVF + 131072;
constexpr int WS_XF = WS_PF + 65536;
constexpr int WS_CS1 = WS_XF + 32768;
constexpr int WS_WQ = WS_CS1 + 32768;
constexpr int WS_WKV = WS_WQ + 448 * 1024;
constexpr int WS_WO = WS_WKV + 56 * 1024;
constexpr int WS_W1 = WS_WO + 448 * 1024;
constexpr int WS_W2 = WS_W1 + 1568 * 1024;
constexpr int WS_KV = WS_W2 + 1568 * 1024;
constexpr int WS_PART = WS_KV + 2 * 256 * 1024;
constexpr int PART_BYTES = 64 * 4 * 28 * 1024;
constexpr int WS_END = WS_PART + 4 * PART_BYTES;
constexpr int L_X = 0;
constexpr int L_ST = 28672;
constexpr int L_Q2 = 30720;
constexpr int L_MISC = 31744;
constexpr int L_QO = 32768;
constexpr int L_SCR = 40960;
constexpr int L_H = L_SCR;
constexpr int L_CS = L_SCR + 16384 + 14336;
constexpr int L_B1 = L_SCR + 32768 + 14336;
constexpr int L_B2 = L_SCR + 49152 + 14336;
constexpr int L_WRES = L_SCR + 65536;
constexpr int LDS_BYTES = L_WRES + 57344;
constexpr int L_RAWKV = 32768;
constexpr int L_RAWQ = L_RAWKV + 25600;
static_assert(LDS_BYTES <= 163840 && L_RAWQ + 100352 + 16 <= LDS_BYTES, "LDS");

#define STAMP(id)
#define STAMP_DECL
#define STAMP_FLUSH()

struct Params {
  const u16 *x, *wq, *wk, *wv, *wo, *w1, *b1, *w2, *b2;
  u16* out;
  unsigned char* ws;
};

DEVINL f32x16 mfma32(v4u a, v4u b, f32x16 c) { return __builtin_amdgcn_mfma_f32_32x32x16_bf16(__builtin_bit_cast(bf16x8, a), __builtin_bit_cast(bf16x8, b), c, 0, 0, 0); }
DEVINL f32x4 mfma16(v4u a, v4u b, f32x4 c) { return __builtin_amdgcn_mfma_f32_16x16x32_bf16(__builtin_bit_cast(bf16x8, a), __builtin_bit_cast(bf16x8, b), c, 0, 0, 0); }
DEVINL void mma(f32x4& acc, v4u a, v4u b) { asm volatile("v_mfma_f32_16x16x32_bf16 %0, %1, %2, %0" : "+v"(acc) : "v"(a), "v"(b)); }
DEVINL void mma0(f32x4& acc, v4u a, v4u b) { asm volatile("v_mfma_f32_16x16x32_bf16 %0, %1, %2, 0" : "=&v"(acc) : "v"(a), "v"(b)); }
DEVINL void mmax(bool first, f32x4& acc, v4u a, v4u b) { if (first) mma0(acc, a, b); else mma(acc, a, b); }
#define MMA_PRE() asm volatile("s_nop 1")
#define MMA_POST2(x, y) do { SBAR(); asm volatile("s_nop 7" : "+v"(x), "+v"(y)); SBAR(); } while (0)
DEVINL f32x16 smf(v4u a, f32x8 b, f32x16 c, int idx) { return __builtin_amdgcn_smfmac_f32_32x32x32_bf16(__builtin_bit_cast(bf16x8, a), __builtin_bit_cast(bf16x16, b), c, idx, 0, 0); }
DEVINL unsigned pk2(float lo, float hi) { f32x2 v = {lo, hi}; return __builtin_bit_cast(unsigned, __builtin_convertvector(v, bf16x2)); }
DEVINL v2u pk4(f32x4 v) { return v2u{pk2(v[0], v[1]), pk2(v[2], v[3])}; }
DEVINL v4u pk8(f32x4 a, f32x4 b) { return v4u{pk2(a[0], a[1]), pk2(a[2], a[3]), pk2(b[0], b[1]), pk2(b[2], b[3])}; }
DEVINL float bflo(unsigned u) { return __uint_as_float(u << 16); }
DEVINL float bfhi(unsigned u) { return __uint_as_float(u & 0xffff0000u); }
DEVINL f32x4 up4(unsigned a, unsigned b) { return f32x4{bflo(a), bfhi(a), bflo(b), bfhi(b)}; }
DEVINL float swap32_add(float v) { auto r = __builtin_amdgcn_permlane32_swap(__float_as_uint(v), __float_as_uint(v), false, false); return __uint_as_float(r[0]) + __uint_as_float(r[1]); }
DEVINL float swap16_add(float v) { auto r = __builtin_amdgcn_permlane16_swap(__float_as_uint(v), __float_as_uint(v), false, false); return __uint_as_float(r[0]) + __uint_as_float(r[1]); }
DEVINL float swap32_max(float v) { auto r = __builtin_amdgcn_permlane32_swap(__float_as_uint(v), __float_as_uint(v), false, false); return fmaxf(__uint_as_float(r[0]), __uint_as_float(r[1])); }
DEVINL float swap16_max(float v) { auto r = __builtin_amdgcn_permlane16_swap(__float_as_uint(v), __float_as_uint(v), false, false); return fmaxf(__uint_as_float(r[0]), __uint_as_float(r[1])); }
DEVINL float red_g(float v) { return swap32_add(swap16_add(v)); }
DEVINL float red_g2(float a, float b) { auto r = __builtin_amdgcn_permlane16_swap(__float_as_uint(a), __float_as_uint(b), false, false); return swap32_add(__uint_as_float(r[0]) + __uint_as_float(r[1])); }
template <int CTRL> DEVINL float dpp(float x) { return __int_as_float(__builtin_amdgcn_mov_dpp(__float_as_int(x), CTRL, 0xf, 0xf, true)); }
DEVINL float row_max(float v) { v = fmaxf(v, dpp<0xB1>(v)); v = fmaxf(v, dpp<0x4E>(v)); v = fmaxf(v, dpp<0x141>(v)); v = fmaxf(v, dpp<0x128>(v)); return v; }
DEVINL float oct_sum(float v) { v += dpp<0xB1>(v); v += dpp<0x4E>(v); v += dpp<0x141>(v); return v; }
typedef __amdgpu_buffer_rsrc_t rsrc_t;
DEVINL rsrc_t mkrs(const void* p, int bytes) { return __builtin_amdgcn_make_buffer_rsrc((void*)p, 0, bytes, 0x00020000); }
DEVINL v4u mkrs4(const void* p, int bytes) { return v4u{(unsigned)(size_t)p, (unsigned)((size_t)p >> 32), (unsigned)bytes, 0x00020000u}; }
template <int AUX> DEVINL v4u ldf(rsrc_t rs, int voff, int soff) { return __builtin_amdgcn_raw_buffer_load_b128(rs, voff, soff, AUX); }
DEVINL void st128(v4u v, rsrc_t rs, int voff, int soff) { __builtin_amdgcn_raw_buffer_store_b128(v, rs, voff, soff, SC1); asm volatile("s_nop 1" :: "v"(v)); }
DEVINL void st64(v2u v, rsrc_t rs, int voff, int soff) { __builtin_amdgcn_raw_buffer_store_b64(v, rs, voff, soff, SC1); }
DEVINL v4u ldrun(rsrc_t rs, int l16, int base, int f) { return __builtin_amdgcn_raw_buffer_load_b128(rs, l16 + (f & 3) * 1024, base + (f >> 2) * 4096, SC1); }
DEVINL float opq_f(float v) { asm("" : "+v"(v)); return v; }
DEVINL int opq_s(int v) { asm volatile("" : "+s"(v)); return v; }
DEVINL int opq_v(int v) { asm volatile("" : "+v"(v)); return v; }
DEVINL v4u lds128(LAS unsigned char* L, int off) { return *(LAS v4u*)(L + off); }
DEVINL f32x4 ldsf4(LAS unsigned char* L, int off) { return *(LAS f32x4*)(L + off); }
DEVINL v2u ldtr(LAS unsigned char* L, int off) { return __builtin_bit_cast(v2u, __builtin_amdgcn_ds_read_tr16_b64_v4i16((LAS s16x4*)(L + off))); }
DEVINL void dma1(v4u rs, int voff, int soff, unsigned ldsbase) {
  unsigned keep;
  asm volatile("s_mov_b32 %0, m0\n\ts_mov_b32 m0, %4\n\ts_nop 0\n\tbuffer_load_dwordx4 %1, %2, %3 offen lds\n\ts_mov_b32 m0, %0" : "=&s"(keep) : "v"(voff), "s"(rs), "s"(soff), "s"(ldsbase) : "memory");
}
DEVINL void touch(v4u rs, int voff, int soff, unsigned ldsbase) {
  unsigned keep;
  asm volatile("s_mov_b32 %0, m0\n\ts_mov_b32 m0, %4\n\ts_nop 0\n\tbuffer_load_dword %1, %2, %3 offen sc1 lds\n\ts_mov_b32 m0, %0" : "=&s"(keep) : "v"(voff), "s"(rs), "s"(soff), "s"(ldsbase) : "memory");
}
DEVINL void lds_wait_ge(int addr, unsigned want) {
  unsigned v, sv;
  asm volatile("1:\n\tds_read_b32 %0, %2\n\ts_waitcnt lgkmcnt(0)\n\tv_readfirstlane_b32 %1, %0\n\ts_nop 0\n\ts_cmp_lt_u32 %1, %3\n\ts_cbranch_scc1 1b" : "=&v"(v), "=&s"(sv) : "v"(addr), "s"(want) : "memory", "scc");
}
constexpr unsigned ONE2 = 0x3f803f80u;
#define ZERO4 (f32x4{0.f, 0.f, 0.f, 0.f})
#define EX(x) __builtin_amdgcn_exp2f(x)
constexpr float QSCALE = 0.18898223650461362f * 1.4426950408889634f;
constexpr float BOUND_MAX = 40.f;
constexpr int POLLW = 6;
constexpr unsigned SPIN_CAP = 1u << 18;
#define PSLEEP 1
#define ROUNDS2 0
#define PFDELAY 40
#define PACE 2

DEVINL bool arrive_last(LAS unsigned char* L, int word, unsigned want, int lane) {
  asm volatile("s_waitcnt vmcnt(0) lgkmcnt(0)" ::: "memory");
  unsigned old = 0;
  if (lane == 0) old = __hip_atomic_fetch_add((LAS unsigned*)(L + L_MISC + 16 + word * 4), 1u, __ATOMIC_RELAXED, __HIP_MEMORY_SCOPE_WORKGROUP);
  return __builtin_amdgcn_readfirstlane(old) == want - 1;
}
DEVINL f32x4 ln_fix(f32x4 a, f32x4 cs, float nmr, float r) {
  f32x4 y;
  UNROLL for (int j = 0; j < 4; ++j) y[j] = opq_f(__builtin_fmaf(a[j], r, cs[j] * nmr));
  return y;
}
DEVINL float sq4(f32x4 v) { return __builtin_fmaf(v[3], v[3], __builtin_fmaf(v[2], v[2], __builtin_fmaf(v[1], v[1], v[0] * v[0]))); }
struct Stat1 {
  f32x4 a; float s, q, mu, rs;
  DEVINL void step(int k) {
    if (k == 0) { s = opq_f(a[0] + a[2]); q = opq_f(a[1] + a[3]); }
    if (k == 1) { s = swap16_add(s); q = swap16_add(q); }
    if (k == 2) { s = swap32_add(s); q = swap32_add(q); }
    if (k == 3) { mu = opq_f(s * (1.0f / DM)); q = __builtin_fmaf(q, 1.0f / DM, -mu * mu); }
    if (k == 4) { rs = __builtin_amdgcn_rsqf(fmaxf(q, 0.f) + 1e-5f); PIN(rs); }
    if (k < 3) { PIN(s); PIN(q); } else if (k == 3) { PIN(mu); PIN(q); }
  }
};
template <bool CS> DEVINL void q_proj(const v4u (&wq)[14], f32x4& cs, LAS unsigned char* L, int w, int lane, int xo) {
  f32x4 a0, a1;
  Stat1 s0, s1;
  v4u b[4][2];
  v4u ones = {ONE2, ONE2, ONE2, ONE2};
  const int so = L_ST + (lane & 15) * 64 + (lane >> 4) * 16;
  UNROLL for (int pp = 0; pp < 3; ++pp) { b[pp][0] = lds128(L, L_X + xo + pp * 2048); b[pp][1] = lds128(L, L_X + xo + pp * 2048 + 256); }
  s0.a = ldsf4(L, so); s1.a = ldsf4(L, so + 1024);
  if (CS) { PIN(ones); MMA_PRE(); }
  SBAR();
  UNROLL for (int pp = 0; pp < 14; ++pp) {
    if (pp + 3 < 14) { b[(pp + 3) % 4][0] = lds128(L, L_X + xo + (pp + 3) * 2048); b[(pp + 3) % 4][1] = lds128(L, L_X + xo + (pp + 3) * 2048 + 256); }
    mmax(pp == 0, a0, wq[pp], b[pp % 4][0]); mmax(pp == 0, a1, wq[pp], b[pp % 4][1]);
    if (CS) mmax(pp == 0, cs, wq[pp], ones);
    SBAR();
    if (pp >= 2 && pp < 12) { if (pp & 1) s1.step((pp - 2) >> 1); else s0.step((pp - 2) >> 1); SBAR(); }
  }
  const float r0 = s0.rs * QSCALE, r1 = s1.rs * QSCALE, n0 = -s0.mu * r0, n1 = -s1.mu * r1;
  if (CS) { SBAR(); asm volatile("s_nop 7" : "+v"(a0), "+v"(a1), "+v"(cs)); SBAR(); } else MMA_POST2(a0, a1);
  f32x4 q0 = ln_fix(a0, cs, n0, r0), q1 = ln_fix(a1, cs, n1, r1);
  if ((w & 1) && lane >= 48) { q0 = ZERO4; q1 = ZERO4; }
  LAS unsigned char* d = L + L_QO + (w >> 1) * 2048 + (w & 1) * 8 + xo;
  *(LAS v2u*)d = pk4(q0); *(LAS v2u*)(d + 256) = pk4(q1);
  const float q2 = red_g2(sq4(q0), sq4(q1));
  if (lane < 32) ((LAS float*)(L + L_Q2))[w * 32 + lane] = q2;
}
template <bool ISV, bool CS> DEVINL void kv_produce(const v4u (&wf)[14], f32x4& cs, LAS unsigned char* L, rsrc_t rs, int layer, int grp, int wr, int lane, int xo) {
  const int n = lane & 15, g = lane >> 4, dt = wr & 1, tt = wr >> 1, scr = L_MISC + 256 + wr * 128;
  f32x4 a, m4, r4;
  Stat1 st;
  v4u b[7];
  v4u ones = {ONE2, ONE2, ONE2, ONE2};
  st.a = ldsf4(L, L_ST + tt * 1024 + n * 64 + g * 16);
  UNROLL for (int pp = 0; pp < 6; ++pp) b[pp] = lds128(L, L_X + xo + tt * 256 + pp * 2048);
  const int vo = ISV ? 2048 + (g >> 1) * 1024 + (16 * dt + n + 32 * (g & 1)) * 16 + tt * 8 : (g >> 1) * 1024 + (8 * (n >> 2) + 4 * tt + (n & 3) + 32 * (g & 1)) * 16 + dt * 8;
  if (CS) { PIN(ones); MMA_PRE(); }
  SBAR();
  UNROLL for (int pp = 0; pp < 14; ++pp) {
    if (pp + 6 < 14) b[(pp + 6) % 7] = lds128(L, L_X + xo + tt * 256 + (pp + 6) * 2048);
    if (ISV) { mmax(pp == 0, a, b[pp % 7], wf[pp]); if (CS) mmax(pp == 0, cs, ones, wf[pp]); }
    else { mmax(pp == 0, a, wf[pp], b[pp % 7]); if (CS) mmax(pp == 0, cs, wf[pp], ones); }
    SBAR();
    if (pp < 5) { st.step(pp); SBAR(); }
    if (ISV && pp == 5) { ((LAS float*)(L + scr))[lane & 31] = (lane & 16) ? st.rs : -st.mu * st.rs; SBAR(); }
    if (ISV && pp == 8) { m4 = ldsf4(L, scr + g * 16); r4 = ldsf4(L, scr + 64 + g * 16); SBAR(); }
  }
  const float nmr = -st.mu * st.rs;
  SBAR(); asm volatile("s_nop 7" : "+v"(a), "+v"(cs)); SBAR();
  f32x4 y;
  if (ISV) { UNROLL for (int j = 0; j < 4; ++j) y[j] = opq_f(__builtin_fmaf(a[j], r4[j], cs[j] * m4[j])); } else y = ln_fix(a, cs, nmr, st.rs);
  v2u o = pk4(y);
  const bool pad = ISV ? (dt && n >= 12) : (dt && g == 3);
  if (pad) o = ISV ? (n == 12 ? v2u{ONE2, ONE2} : v2u{0u, 0u}) : v2u{ONE2, 0x00003f80u};
  st64(o, rs, vo, WS_KV + (layer * 64 + grp) * 4096);
  SBAR();
  if (!ISV) {
    const float q2 = row_max(red_g(pad ? 0.f : sq4(y)));
    if (lane == 0) *(LAS float*)(L + L_MISC + 32 + wr * 4) = q2;
  }
}
DEVINL void kv_signal(LAS unsigned char* L, rsrc_t rs, int layer, int grp, int isv, int lane, unsigned tlo, unsigned thi) {
  tlo = opq_s(tlo); thi = opq_s(thi);
  if (arrive_last(L, 1, 4 * (layer + 1), lane)) {
    const f32x4 q = ldsf4(L, L_MISC + 32);
    const unsigned pay = isv ? 0u : pk2(0.f, fmaxf(opq_f(q[0] + q[1]), opq_f(q[2] + q[3])) * 1.01f);
    if (lane < 8) st64(v2u{tlo, thi | pay}, rs, lane * 8192, WS_KVF + layer * 65536 + grp * 128 + isv * 8);
  }
}
DEVINL void signal_and_wait(rsrc_t rs, int ex, int grp, int c, int lane, unsigned tlo, unsigned thi) {
  tlo = opq_s(tlo); thi = opq_s(thi);
  if (lane == 0) st64(v2u{tlo, thi}, rs, 0, WS_XF + (ex * 64 + grp) * 128 + c * 8);
_Pragma("unroll 1")
  for (unsigned spins = 0; spins < SPIN_CAP; ++spins) {
    asm volatile("" ::: "memory");
    const v2u f = __builtin_amdgcn_raw_buffer_load_b64(rs, (lane & 3) * 8, WS_XF + (ex * 64 + grp) * 128, SC1);
    if (__all(f[0] == tlo && f[1] == thi)) break;
    __builtin_amdgcn_s_sleep(PSLEEP);
  }
  asm volatile("" ::: "memory");
}
DEVINL void make_sel(v4u& s0, v4u& s1, int lane) {
  const int n = lane & 15, g = lane >> 4;
  const unsigned one = (n >> 2) == g ? ((n & 1) ? 0x3f800000u : 0x00003f80u) : 0u;
  const unsigned a = (n & 2) ? 0u : one, b = (n & 2) ? one : 0u;
  s0 = v4u{a, b, 0u, 0u}; s1 = v4u{0u, 0u, a, b};
  PIN(s0); PIN(s1);
}
DEVINL f32x2 pkfma(f32x2 a, f32x2 b, f32x2 c) { return __builtin_elementwise_fma(a, b, c); }
DEVINL f32x2 dup2(float v) { f32x2 r = {v, v}; asm("" : "+v"(r)); return r; }
DEVINL float gelu_s(float a, float k1, float k0, float ir) {
  const float z = a * __builtin_fmaf(a * a, k1, k0);
  return opq_f(a * __builtin_amdgcn_rcpf(__builtin_fmaf(__builtin_amdgcn_exp2f(z), ir, ir)));
}
DEVINL unsigned gelu_p(f32x2 a, f32x2 k1, f32x2 k0, f32x2 ir) {
  const f32x2 z = a * pkfma(a * a, k1, k0);
  const f32x2 d = pkfma(f32x2{__builtin_amdgcn_exp2f(z[0]), __builtin_amdgcn_exp2f(z[1])}, ir, ir);
  const f32x2 y = a * f32x2{__builtin_amdgcn_rcpf(d[0]), __builtin_amdgcn_rcpf(d[1])};
  return pk2(y[0], y[1]);
}

template <bool ATT> DEVINL void wave_main(const Params& p, LAS unsigned char* L, const int grp_, const int c_, const int w_, const int lane_) {
  constexpr int NT = ATT ? 3 : 4;
  constexpr int APRE = 10;
  constexpr int RING = 44;
  constexpr int N1 = ATT ? 28 : 56;
  constexpr int NS = N1 + 14 * NT;
  STAMP_DECL;
  STAMP(0);
  const int wr_ = w_ & 3, t0_ = ATT ? 16 + 3 * wr_ : 4 * wr_;
  const int l16_ = lane_ * 16, xo_ = (lane_ >> 4) * 512 + (lane_ & 15) * 16;
  const int xa_ = xo_ + (ATT ? 8 + wr_ : 2 * wr_) * 2048, xb_ = xo_ + (12 + (wr_ >> 1)) * 2048 + (wr_ & 1) * 8;
  const int w1b_ = WS_W1 + (392 * c_ + 14 * t0_ + (ATT ? 14 : 0)) * 1024, w2b_ = WS_W2 + (392 * c_ + 14 * t0_) * 1024, wob_ = WS_WO + (28 * c_ + t0_) * 4096;
  const int pb_ = WS_PART + (grp_ * 112 + t0_) * 1024;
  int lane = lane_, grp = grp_, c = c_, w = w_, wr = wr_, l16 = l16_, xo = xo_, xa = xa_, xb = xb_, w1b = w1b_, w2b = w2b_, wob = wob_, pb = pb_;
#define FRESH() do { lane = opq_v(lane_); l16 = opq_v(l16_); xo = opq_v(xo_); xa = opq_v(xa_); if (ATT) xb = opq_v(xb_); grp = opq_s(grp_); c = opq_s(c_); w = opq_s(w_); wr = opq_s(wr_); \
    w1b = opq_s(w1b_); w2b = opq_s(w2b_); wob = opq_s(wob_); pb = opq_s(pb_); } while (0)
#define FT(i) (ATT ? ((i) < 2 ? 16 + 2 * wr + (i) : 24 + wr) : 4 * wr + (i))
  const rsrc_t rs = mkrs(p.ws, WS_END);
  const v4u ones = {ONE2, ONE2, ONE2, ONE2};
#define SLOAD(s) do { if ((s) < NS) ring[(s) % RING] = (s) < N1 ? ldrun(rs, l16, w1b, (s)) : ldrun(rs, l16, w2b, (s) - N1); } while (0)
#define SRANGE(a, b) do { UNROLL for (int s_ = (a); s_ < (b); ++s_) SLOAD(s_); SBAR(); } while (0)
#define WOF(i, h) ldrun(rs, l16, wob, (i) * 4 + (h))
  const int kvrole = c == (grp & 3) ? 0 : c == ((grp + 1) & 3) ? 1 : 2;
#define csq_o ((ATT ? L_CS : L_B1) + 1792 + wr * 64 + (lane >> 4) * 16)
  unsigned tlo, thi;
  f32x4 acc[NT][2];

  f32x4 cq, ckv = ZERO4;
  v4u pre[4];
  const int unit = wr_ * 256 + blockIdx.x;
  int udst[4], tr0, tr1;
  {
    const v2u ep = __builtin_amdgcn_raw_buffer_load_b64(rs, 0, WS_EPOCH, SC1);
    v4u wq[14];
    if (ATT) {
      const int tid = threadIdx.x, tok = tid >> 3, sub = tid & 7;
      const GAS v4u* src = (const GAS v4u*)(p.x + (32 * grp + tok) * DM);
      v4u v[7];
      UNROLL for (int i = 0; i < 7; ++i) v[i] = src[sub + 8 * i];
      if (kvrole < 2) {
        const v4u r4 = mkrs4(kvrole ? p.wv : p.wk, 25088);
        UNROLL for (int i = 0; i < 7; ++i) if (i < 6 || wr == 0) dma1(r4, l16, (4 * i + wr) * 1024, (unsigned)(size_t)L + L_RAWKV + (4 * i + wr) * 1024);
      }
      if (tid == 0) { *(LAS v4u*)(L + L_MISC + 16) = v4u{0, 0, 0, 0}; *(LAS unsigned*)(L + L_MISC + 48) = 0u; }
      BARRIER();
      float s1 = 0.f, s2 = 0.f;
      UNROLL for (int i = 0; i < 7; ++i) {
        const int m = sub + 8 * i, ft = m >> 1;
        LAS unsigned char* d = L + L_X + (ft >> 1) * 2048 + (m & 1) * 1024 + tok * 16 + (ft & 1) * 8;
        *(LAS v2u*)d = v2u{v[i][0], v[i][1]};
        *(LAS v2u*)(d + 512) = v2u{v[i][2], v[i][3]};
        UNROLL for (int k = 0; k < 4; ++k) {
          s1 = __builtin_amdgcn_fdot2_f32_bf16(__builtin_bit_cast(bf16x2, (unsigned)v[i][k]), __builtin_bit_cast(bf16x2, ONE2), s1, false);
          s2 = __builtin_amdgcn_fdot2_f32_bf16(__builtin_bit_cast(bf16x2, (unsigned)v[i][k]), __builtin_bit_cast(bf16x2, (unsigned)v[i][k]), s2, false);
        }
      }
      s1 = oct_sum(s1); s2 = oct_sum(s2);
      *(LAS f32x2*)(L + L_ST + tid * 8) = sub == 0 ? f32x2{s1, s2} : f32x2{0.f, 0.f};
      tlo = __builtin_amdgcn_readfirstlane(ep[0]) + 1u; thi = __builtin_amdgcn_readfirstlane(ep[1]) & 0xffffu;
      STAMP(28);
      if (kvrole < 2) {
        arrive_last(L, 2, 4, lane);
        lds_wait_ge((int)(size_t)L + L_MISC + 24, 4u);
        v4u wf[14];
        const int ro = L_RAWKV + ((lane >> 4) * 4 + ((lane & 15) >> 2)) * 56 + (16 * (wr & 1) + 4 * (lane & 3)) * 2;
        UNROLL for (int pp = 0; pp < 14; ++pp) { const v2u a = ldtr(L, ro + pp * 32 * 56), b = ldtr(L, ro + (pp * 32 + 16) * 56); wf[pp] = v4u{a[0], a[1], b[0], b[1]}; }
        if (kvrole) kv_produce<true, true>(wf, ckv, L, rs, 0, grp, wr, lane, xo); else kv_produce<false, true>(wf, ckv, L, rs, 0, grp, wr, lane, xo);
        STAMP(27);
        kv_signal(L, rs, 0, grp, kvrole, lane, tlo, thi);
        if (grp == 63 && wr < 2) { UNROLL for (int pp = 0; pp < 14; ++pp) st128(wf[pp], rs, l16, WS_WKV + ((kvrole * 2 + wr) * 14 + pp) * 1024); }
      }
      STAMP(29);
    } else {
      const v4u r4 = mkrs4(p.wq, DM * DM * 2);
      const int m0 = 64 * wr + lane, m1 = m0 + 256;
      const int v0 = (m0 / 14) * 896 + (m0 % 14) * 16, v1 = (m1 / 14) * 896 + (m1 % 14) * 16;
      int blk = (blockIdx.x >> 5) * 2;
      __builtin_amdgcn_s_barrier();
      const u16* src; int stride, rows = 32;
      if (unit < 392) { src = p.w1 + (unit / 28) * 32 * FF + (unit % 28) * 64; stride = FF; }
      else if (unit < 784) { src = p.w2 + ((unit - 392) / 7) * 32 * DM + ((unit - 392) % 7) * 64; stride = DM; }
      else if (unit < 896) { src = p.wo + ((unit - 784) / 7) * DH * DM + ((unit - 784) % 7) * 64; stride = DM; rows = 28; }
      else { src = p.w1 + ((unit - 896) / 28) * 112 * FF + ((unit - 896) % 28) * 64; stride = FF; rows = unit < 1008 ? 112 : 0; }
      const rsrc_t ru = mkrs(src, rows ? ((rows - 1) * stride + 64) * 2 : 0);
      const int vo = ((lane >> 3) * stride + (lane & 7) * 8) * 2;
      v4u cp[14];
      if (unit >= 896) { UNROLL for (int i = 0; i < 14; ++i) cp[i] = ldf<0>(ru, vo, i * 8 * stride * 2); }
      SBAR();
      UNROLL for (int i = 0; i < 14; ++i) {
        blk = blk >= 14 ? blk - 14 : blk;
        dma1(r4, v0, blk * 32 * 896 + c * 224, (unsigned)(size_t)L + L_RAWQ + blk * 7168 + wr * 1024);
        if (wr < 3) dma1(r4, v1, blk * 32 * 896 + c * 224, (unsigned)(size_t)L + L_RAWQ + blk * 7168 + (wr + 4) * 1024);
        ++blk;
      }
      tlo = __builtin_amdgcn_readfirstlane(ep[0]) + 1u; thi = __builtin_amdgcn_readfirstlane(ep[1]) & 0xffffu;
      if (unit >= 896 && unit < 1008) {
          f32x4 s0 = ZERO4, s1 = ZERO4;
          UNROLL for (int i = 0; i < 14; ++i) { s0 += up4(cp[i][0], cp[i][1]); s1 += up4(cp[i][2], cp[i][3]); }
          UNROLL for (int j = 0; j < 4; ++j) { s0[j] = red_g(s0[j] + dpp<0x128>(s0[j])); s1[j] = red_g(s1[j] + dpp<0x128>(s1[j])); }
          const int dst = WS_CS1 + (((unit - 896) / 28) * FF + ((unit - 896) % 28) * 64) * 4;
          DRAIN();
          if (lane < 8) { st128(__builtin_bit_cast(v4u, s0), rs, lane * 32, dst); st128(__builtin_bit_cast(v4u, s1), rs, lane * 32 + 16, dst); }
      }
      {
        const int g = lane >> 4, q = (lane & 15) >> 2;
        const bool iso = unit >= 784;
        const int r0 = iso ? 16 * (g >> 1) + 4 * (g & 1) + q : 4 * g + q;
        tr0 = r0 * 144 + (lane & 3) * 8; tr1 = tr0 + (iso ? 8 : 16) * 144;
        int cc, pp, cb7;
        if (unit < 392) { pp = unit / 28; cc = (unit % 28) / 7; cb7 = unit % 7; }
        else if (unit < 784) { pp = ((unit - 392) / 7) % 14; cc = (unit - 392) / 98; cb7 = (unit - 392) % 7; }
        else { pp = ((unit - 784) / 7) & 3; cc = (unit - 784) / 28; cb7 = (unit - 784) % 7; }
        UNROLL for (int i = 0; i < 4; ++i) {
          const int t = 4 * cb7 + i;
          const int sb = t < 16 ? (t & ~3) : t < 24 ? 16 + 3 * ((t - 16) >> 1) : 16 + 3 * (t - 24), si = t < 16 ? (t & 3) : t < 24 ? (t & 1) : 2, nt = t < 16 ? 4 : 3;
          udst[i] = opq_s(iso ? WS_WO + ((cc * 28 + sb + si) * 4 + pp) * 1024 : (unit < 392 ? WS_W1 : WS_W2) + (cc * 392 + sb * 14 + pp * nt + si) * 1024);
        }
        tr0 = opq_v(tr0); tr1 = opq_v(tr1);
      }
      if (unit < 896 || unit >= 1008) DRAIN();
      UNROLL for (int i = 0; i < 4; ++i) pre[i] = ldf<0>(ru, vo, i * 8 * stride * 2);
      STAMP(29);
    }
    BARRIER();
    STAMP(30);
    {
      const int ro = L_RAWQ + ((lane >> 4) * 4 + ((lane & 15) >> 2)) * 224 + (DH * (w >> 1) + 16 * (w & 1) + 4 * (lane & 3)) * 2;
      UNROLL for (int pp = 0; pp < 14; ++pp) { const v2u a = ldtr(L, ro + pp * 32 * 224), b = ldtr(L, ro + (pp * 32 + 16) * 224); wq[pp] = v4u{a[0], a[1], b[0], b[1]}; }
    }
    STAMP(31);
    q_proj<true>(wq, cq, L, w, lane, xo);
    UNROLL for (int pp = 0; pp < 14; ++pp) if (grp == pp) st128(wq[pp], rs, l16, WS_WQ + ((8 * c + w) * 14 + pp) * 1024);
    if (ATT) arrive_last(L, 3, 8, lane);
  }

  auto bounds = [&](float (&cb)[4]) {
    const float km2 = *(LAS float*)(L + L_MISC);
    bool unsafe = false;
    UNROLL for (int j = 0; j < 4; ++j) {
      LAS float* q = (LAS float*)(L + L_Q2) + ((wr + 1 + j) & 3) * 64 + (lane & 31);
      cb[j] = __builtin_amdgcn_sqrtf((q[0] + q[32]) * km2) * 1.02f;
      unsafe |= !(cb[j] <= BOUND_MAX);
    }
    return (bool)__any(unsafe);
  };
  auto gate = [&](int layer) {
    STAMP(layer * 32 + 1); FRESH();
    if (!ATT && w == POLLW) {
      v4u f; bool ok = false;
_Pragma("unroll 1")
      for (unsigned spins = 0; spins < SPIN_CAP; ++spins) {
        asm volatile("" ::: "memory");
        if (!ok) { f = ldf<SC1>(rs, lane * 128, WS_KVF + layer * 65536 + (blockIdx.x & 7) * 8192); ok = f[0] == tlo && f[2] == tlo && (f[1] & 0xffffu) == thi && f[3] == thi; }
        if (__all(ok)) break;
        __builtin_amdgcn_s_sleep(PSLEEP);
      }
      asm volatile("" ::: "memory");
      const float km = swap32_max(swap16_max(row_max(bfhi(f[1]))));
      if (lane == 0) *(LAS float*)(L + L_MISC) = km;
      if (layer == 1 && blockIdx.x == 0 && lane == 0) __builtin_amdgcn_raw_buffer_store_b32(opq_s(tlo), rs, 0, WS_EPOCH, SC1);
    }
    BARRIER();
    if (!ATT) { float cb[4]; if (__builtin_expect(bounds(cb), 0)) { BARRIER(); BARRIER(); } }
    STAMP(layer * 32 + 2);
  };
  gate(0);
  if ((lane & 15) == 0) *(LAS f32x4*)(L + csq_o) = cq;
  if (ATT && wr < 2) { if (kvrole == 1) { if (lane < 16) ((LAS float*)(L + L_B2 + 1792 + wr * 64))[lane] = ckv[0]; } else if ((lane & 15) == 0) *(LAS f32x4*)(L + L_B2 + 1792 + wr * 64 + (lane >> 4) * 16) = ckv; }
  if (!ATT) {
      {
        __builtin_amdgcn_s_setprio(3);
        if (unit < 896) {
          LAS unsigned char* T = L + L_SCR + wr * 4608;
          UNROLL for (int i = 0; i < 4; ++i) *(LAS v4u*)(T + (8 * i + (lane >> 3)) * 144 + (lane & 7) * 16) = pre[i];
          UNROLL for (int i = 0; i < 4; ++i) {
            const v2u a = ldtr(T, tr0 + 32 * i), b = ldtr(T, tr1 + 32 * i);
            st128(v4u{a[0], a[1], b[0], b[1]}, rs, l16, udst[i]);
          }
        }
        if (arrive_last(L, 3, 8, lane) && lane < 8) st64(v2u{tlo, thi}, rs, lane * 8192, WS_PF + grp * 128 + c * 8);
        STAMP(22);
        if (w == POLLW) {
          bool ok = false;
          __builtin_amdgcn_s_sleep(PFDELAY);
_Pragma("unroll 1")
          for (unsigned spins = 0; spins < SPIN_CAP; ++spins) {
            asm volatile("" ::: "memory");
            if (!ok) { const v4u f0 = ldf<SC1>(rs, lane * 128, WS_PF + (blockIdx.x & 7) * 8192), f1 = ldf<SC1>(rs, lane * 128 + 16, WS_PF + (blockIdx.x & 7) * 8192);
              ok = f0[0] == tlo && f0[2] == tlo && f1[0] == tlo && f1[2] == tlo && f0[1] == thi && f0[3] == thi && f1[1] == thi && f1[3] == thi; }
            if (__all(ok)) break;
            __builtin_amdgcn_s_sleep(4);
          }
          if (lane == 0) __hip_atomic_store((LAS unsigned*)(L + L_MISC + 24), 0x100u, __ATOMIC_RELAXED, __HIP_MEMORY_SCOPE_WORKGROUP);
        } else {
_Pragma("unroll 1")
          for (unsigned spins = 0; spins < SPIN_CAP * 8; ++spins) {
            if (__hip_atomic_load((LAS unsigned*)(L + L_MISC + 24), __ATOMIC_RELAXED, __HIP_MEMORY_SCOPE_WORKGROUP) == 0x100u) break;
            __builtin_amdgcn_s_sleep(2);
          }
        }
        asm volatile("" ::: "memory");
        if (lane == 0) __hip_atomic_fetch_add((LAS unsigned*)(L + L_MISC + 48), 1u, __ATOMIC_RELAXED, __HIP_MEMORY_SCOPE_WORKGROUP);
        STAMP(23);
        if (lane < 28) {
          const int e = (wr * 28 + lane) * 4;
          f32x4 s = ZERO4;
          UNROLL for (int k = 0; k < 4; ++k) s += __builtin_bit_cast(f32x4, ldf<SC1>(rs, e * 4, WS_CS1 + (k * FF + 448 * c) * 4));
          const v2u b1 = *(const GAS v2u*)(p.b1 + 448 * c + e), b2 = *(const GAS v2u*)(p.b2 + e);
          *(LAS f32x4*)(L + L_CS + e * 4) = s; *(LAS f32x4*)(L + L_B1 + e * 4) = up4(b1[0], b1[1]); *(LAS f32x4*)(L + L_B2 + e * 4) = up4(b2[0], b2[1]);
        }
        v4u t[14];
        UNROLL for (int f = 0; f < 14; ++f) { t[f] = ldrun(rs, l16, WS_W1 + (392 * c + 14 * (16 + 3 * wr)) * 1024, f); __builtin_amdgcn_s_sleep(1); SBAR(); }
        UNROLL for (int f = 0; f < 14; ++f) { *(LAS v4u*)(L + L_WRES + (wr * 14 + f) * 1024 + l16) = t[f]; }
        __builtin_amdgcn_s_setprio(0);
      }
  }
#pragma unroll 1
  for (int layer = 0;; ++layer) {
    FRESH();
    v4u wo[NT][4];
    v4u ring[RING];
    f32x4 a0, a1; float l;
    if (ATT) {
      __builtin_amdgcn_s_setprio(2);
      const int kvb = WS_KV + (layer * 64 + wr * 16) * 4096;
#define LD(t, f) ldf<SC1>(rs, l16 + (f) * 1024, kvb + (t) * 4096)
      v4u k0 = LD(0, 0), k1 = LD(0, 1), v0 = LD(0, 2), v1 = LD(0, 3);
      v4u k0n = LD(1, 0), k1n = LD(1, 1), v0n = LD(1, 2), v1n = LD(1, 3);
      SBAR();
      v4u Q0[4], Q1[4];
      float cb[4];
      UNROLL for (int j = 0; j < 4; ++j) { const int o = L_QO + ((wr + 1 + j) & 3) * 2048 + l16; Q0[j] = lds128(L, o); Q1[j] = lds128(L, o + 1024); }
      const f32x16 zero = {};
      if (__builtin_expect(bounds(cb), 0)) {
        LAS float* mx = (LAS float*)(L + L_SCR + 20480);
        UNROLL for (int j = 0; j < 4; ++j) {
          float m = -INFINITY;
#pragma unroll 1
          for (int t = 0; t < 16; ++t) {
            f32x16 s = mfma32(LD(t, 0), Q0[j], zero); s = mfma32(LD(t, 1), Q1[j], s);
            UNROLL for (int r = 0; r < 16; ++r) m = fmaxf(m, s[r]);
          }
          m = swap32_max(m);
          if (lane < 32) mx[(((wr + 1 + j) & 3) * 4 + wr) * 32 + lane] = m;
        }
        BARRIER();
        UNROLL for (int j = 0; j < 4; ++j) {
          float m = -INFINITY;
          UNROLL for (int ww = 0; ww < 4; ++ww) m = fmaxf(m, mx[(((wr + 1 + j) & 3) * 4 + ww) * 32 + (lane & 31)]);
          cb[j] = m;
        }
        BARRIER();
      }
      UNROLL for (int j = 0; j < 4; ++j) {
        const float cc = -cb[j];
        const unsigned c0 = pk2(cc, 0.f) & 0xffffu; const float r1 = cc - bflo(c0);
        const unsigned c1 = pk2(r1, 0.f) & 0xffffu; const float r2 = r1 - bflo(c1);
        const unsigned c2 = pk2(r2, 0.f) & 0xffffu;
        Q1[j][2] = lane >= 32 ? (c0 | (c1 << 16)) : Q1[j][2]; Q1[j][3] = lane >= 32 ? c2 : Q1[j][3];
      }
      STAMP(layer * 32 + 3);
      int idx = 0xdddd; PIN(idx);
      f32x16 O[4] = {};
      v4u k0m, k1m, v0m, v1m, v1p;
      f32x16 S, Sn; f32x8 Phi;
#define ODUMP(j, k) *(LAS f32x4*)(L + L_SCR + ((wr + 1 + (j)) & 3) * 16384 + (j) * 4096 + (k) * 1024 + l16) = f32x4{O[j][4 * (k)], O[j][4 * (k) + 1], O[j][4 * (k) + 2], O[j][4 * (k) + 3]}
      S = mfma32(k0, Q0[0], zero); S = mfma32(k1, Q1[0], S);
      UNROLL for (int t = 0; t < 16; ++t) {
        UNROLL for (int h = 0; h < 4; ++h) {
          f32x8 Plo, Ph2;
          const bool more = !(t == 15 && h == 3);
          SBAR();
          if (more) Sn = mfma32(h == 3 ? k0n : k0, Q0[(h + 1) & 3], zero);
          SBAR();
          if (t + 2 < 16) { if (h == 0) k0m = LD(t + 2, 0); if (h == 1) k1m = LD(t + 2, 1); if (h == 2) v0m = LD(t + 2, 2); if (h == 3) v1m = LD(t + 2, 3); }
          else wo[t - 14][h] = WOF(t - 14, h);
          SBAR();
          Plo[0] = EX(S[0]); Plo[1] = EX(S[1]); Plo[2] = EX(S[2]); Plo[3] = EX(S[3]); SBAR();
          if (more) Sn = mfma32(h == 3 ? k1n : k1, Q1[(h + 1) & 3], Sn);
          SBAR();
          if (t == 15) { wo[2][h] = WOF(2, h); SBAR(); }
          if (t >= 9 && t < 14 && (h & 1)) { SLOAD(2 * (t - 9) + (h >> 1)); SBAR(); }
          Plo[4] = EX(S[4]); Plo[5] = EX(S[5]); Plo[6] = EX(S[6]); Plo[7] = EX(S[7]); SBAR();
          if (t || h) O[(h + 3) & 3] = smf(h == 0 ? v1p : v1, Phi, O[(h + 3) & 3], idx);
          SBAR();
          if (t == 15 && h >= 2) { ODUMP(h - 2, 0); ODUMP(h - 2, 1); SBAR(); }
          Ph2[0] = EX(S[8]); Ph2[1] = EX(S[9]); Ph2[2] = EX(S[10]); Ph2[3] = EX(S[11]); SBAR();
          O[h] = smf(v0, Plo, O[h], idx); SBAR();
          if (t == 15 && h >= 2) { ODUMP(h - 2, 2); ODUMP(h - 2, 3); SBAR(); }
          if (t == 8 && h == 3) { lds_wait_ge((int)(size_t)L + L_MISC + 48, 4u * (layer + 1)); SBAR(); }
          Ph2[4] = EX(S[12]); Ph2[5] = EX(S[13]); Ph2[6] = EX(S[14]); Ph2[7] = EX(S[15]); PIN(Ph2); SBAR();
          Phi = Ph2; S = Sn;
        }
        v1p = v1; k0 = k0n; k1 = k1n; v0 = v0n; v1 = v1n;
        k0n = k0m; k1n = k1m; v0n = v0m; v1n = v1m;
      }
      O[3] = smf(v1p, Phi, O[3], idx);
#undef LD
      STAMP(layer * 32 + 4);
      ODUMP(2, 0); ODUMP(2, 1); ODUMP(2, 2); ODUMP(2, 3);
      SBAR(); SRANGE(APRE, APRE + 2);
      ODUMP(3, 0); ODUMP(3, 1);
      ((LAS float*)(L + L_Q2))[wr * 64 + lane] = O[3][12];
      __builtin_amdgcn_s_setprio(0);
      SBAR(); SRANGE(APRE + 2, APRE + 4);
      BARRIER();
      f32x4 r0[3], r1[3];
      UNROLL for (int j = 0; j < 3; ++j) { const int o = L_SCR + wr * 16384 + j * 4096 + 2048 + l16; r0[j] = ldsf4(L, o); r1[j] = ldsf4(L, o + 1024); }
      SBAR(); SRANGE(APRE + 4, APRE + 6);
      a0 = (f32x4{O[3][8], O[3][9], O[3][10], O[3][11]} + r0[0]) + (r0[1] + r0[2]);
      a1 = (f32x4{O[3][12], O[3][13], O[3][14], O[3][15]} + r1[0]) + (r1[1] + r1[2]);
      auto r = __builtin_amdgcn_permlane32_swap(__float_as_uint(a1[0]), __float_as_uint(a1[0]), false, false);
      l = __uint_as_float(r[1]);
    } else {
      UNROLL for (int i = 0; i < 4; ++i) UNROLL for (int h = 0; h < 4; ++h) { wo[i][h] = WOF(i, h); __builtin_amdgcn_s_sleep(1); if (layer) __builtin_amdgcn_s_sleep(PACE); SBAR(); }
      UNROLL for (int s = 0; s < 34; ++s) { SLOAD(s); __builtin_amdgcn_s_sleep(1); if (layer) __builtin_amdgcn_s_sleep(PACE); SBAR(); }
      STAMP(layer * 32 + 4);
      BARRIER();
      f32x4 r0[4], r1[4]; float lv[4];
      UNROLL for (int j = 0; j < 4; ++j) {
        const int o = L_SCR + wr * 16384 + j * 4096;
        r0[j] = ldsf4(L, o + l16); r1[j] = ldsf4(L, o + 1024 + l16);
        lv[j] = j < 3 ? *(LAS float*)(L + o + 3072 + 512 + (l16 & 511)) : ((LAS float*)(L + L_Q2))[wr * 64 + 32 + (lane & 31)];
      }
      a0 = (r0[0] + r0[1]) + (r0[2] + r0[3]); a1 = (r1[0] + r1[1]) + (r1[2] + r1[3]);
      l = (lv[3] + lv[0]) + (lv[1] + lv[2]);
    }
    {
      const float il = __builtin_amdgcn_rcpf(l);
      *(LAS v4u*)(L + L_QO + wr * 2048 + (ATT ? 1024 : 0) + l16) = pk8(a0 * il, a1 * il);
      SBAR();
      if (ATT) SRANGE(APRE + 6, APRE + 7);
      BARRIER();
    }
    STAMP(layer * 32 + 5); FRESH();
    auto add_base = [&](bool bias) {
      v4u xp[2][2]; v2u xs[2]; f32x4 bb[NT];
      UNROLL for (int tt = 0; tt < 2; ++tt) {
        xp[0][tt] = lds128(L, L_X + xa + tt * 256);
        if (ATT) xs[tt] = *(LAS v2u*)(L + L_X + xb + tt * 256); else xp[1][tt] = lds128(L, L_X + xa + 2048 + tt * 256);
      }
      if (bias) { UNROLL for (int i = 0; i < NT; ++i) bb[i] = ldsf4(L, L_B2 + FT(i) * 64 + (lane >> 4) * 16); }
      UNROLL for (int i = 0; i < NT; ++i) UNROLL for (int tt = 0; tt < 2; ++tt) {
        acc[i][tt] += (ATT && i == 2) ? up4(xs[tt][0], xs[tt][1]) : up4(xp[i >> 1][tt][2 * (i & 1)], xp[i >> 1][tt][2 * (i & 1) + 1]);
        if (bias) acc[i][tt] += bb[i];
        PIN(acc[i][tt]);
      }
    };
    auto gather_update = [&](int ex, v4u s0, v4u s1) {
      v4u pr[3][NT];
      UNROLL for (int k = 0; k < 3; ++k) {
        UNROLL for (int i = 0; i < NT; ++i) pr[k][i] = ldf<SC1>(rs, l16 + i * 1024, pb + ex * PART_BYTES + (c ^ (k + 1)) * 28672);
      }
      SBAR();
      UNROLL for (int i = 0; i < NT; ++i) { PIN(acc[i][0]); PIN(acc[i][1]); }
      MMA_PRE();
      UNROLL for (int k = 0; k < 3; ++k) UNROLL for (int i = 0; i < NT; ++i) { mma(acc[i][0], s0, pr[k][i]); mma(acc[i][1], s1, pr[k][i]); }
      SBAR();
      asm volatile("s_nop 3" : "+v"(acc[0][0]), "+v"(acc[0][1]), "+v"(acc[1][0]), "+v"(acc[1][1]));
      if (ATT) asm volatile("s_nop 1" : "+v"(acc[0][0]), "+v"(acc[0][1]), "+v"(acc[1][0]), "+v"(acc[1][1]));
      float a1s[2] = {0.f, 0.f}, a2s[2] = {0.f, 0.f};
      UNROLL for (int h = 0; h < 2; ++h) {
        UNROLL for (int tt = 0; tt < 2; ++tt) {
          v4u x;
          if (ATT && h == 1) { const v2u y = pk4(acc[2][tt]); *(LAS v2u*)(L + L_X + xb + tt * 256) = y; x = v4u{y[0], y[1], 0u, 0u}; }
          else { x = pk8(acc[2 * h][tt], acc[(2 * h + 1) % NT][tt]); *(LAS v4u*)(L + L_X + xa + h * 2048 + tt * 256) = x; }
          UNROLL for (int k = 0; k < ((ATT && h == 1) ? 2 : 4); ++k) {
            a1s[tt] = __builtin_amdgcn_fdot2_f32_bf16(__builtin_bit_cast(bf16x2, (unsigned)x[k]), __builtin_bit_cast(bf16x2, ONE2), a1s[tt], false);
            a2s[tt] = __builtin_amdgcn_fdot2_f32_bf16(__builtin_bit_cast(bf16x2, (unsigned)x[k]), __builtin_bit_cast(bf16x2, (unsigned)x[k]), a2s[tt], false);
          }
        }
        SBAR();
      }
      const float sx = red_g2(a1s[0], a1s[1]), sy = red_g2(a2s[0], a2s[1]);
      if (lane < 32) *(LAS f32x2*)(L + L_ST + lane * 64 + w * 8) = f32x2{sx, sy};
    };
    {
      v4u ob[4][2];
      UNROLL for (int h = 0; h < 4; ++h) { ob[h][0] = lds128(L, L_QO + xo + h * 2048); ob[h][1] = lds128(L, L_QO + xo + h * 2048 + 256); }
      SBAR();
      if (ATT) SRANGE(APRE + 7, APRE + 8);
      const int po = pb + layer * 2 * PART_BYTES + c * 28672;
      UNROLL for (int i = 0; i <= NT; ++i) {
        if (i < NT) {
          UNROLL for (int h = 0; h < 4; ++h) {
            mmax(h == 0, acc[i][0], wo[i][h], ob[h][0]); mmax(h == 0, acc[i][1], wo[i][h], ob[h][1]);
            if (ATT && (h & 1)) { SBAR(); SRANGE(APRE + 8 + 2 * i + (h >> 1), APRE + 9 + 2 * i + (h >> 1)); }
          }
        }
        SBAR();
        if (i > 0) {
          if (i == NT) MMA_POST2(acc[i - 1][0], acc[i - 1][1]);
          st128(pk8(acc[i - 1][0], acc[i - 1][1]), rs, l16 + (i - 1) * 1024, po);
        }
        SBAR();
      }
    }
    STAMP(layer * 32 + 6);
    {
      add_base(false);
      v4u s0, s1;
      make_sel(s0, s1, lane);
      const bool last = arrive_last(L, 0, 8 * (2 * layer + 1), lane);
      STAMP(layer * 32 + 7); FRESH();
      if (last) signal_and_wait(rs, layer * 2, grp, c, lane, tlo, thi);
      STAMP(layer * 32 + 8);
      if (ATT) { UNROLL for (int s = APRE + 14; s < 39; ++s) { SLOAD(s); if (!last) __builtin_amdgcn_s_sleep(1); SBAR(); } }
      else SRANGE(34, 38);
      BARRIER();
      STAMP(layer * 32 + 9); FRESH();
      gather_update(layer * 2, s0, s1);
    }
    SBAR();
    STAMP(layer * 32 + 10);
    BARRIER();
    STAMP(layer * 32 + 11); FRESH();
    {
      v4u b[3][2];
      v4u res[3];
      f32x4 bv[NT], csv[NT];
      Stat1 st[2];
      float k1r[2], k0r[2], irs[2];
      constexpr float G0 = -2.0f * 0.7978845608028654f * 1.4426950408889634f, G1 = G0 * 0.044715f;
      __builtin_amdgcn_s_setprio(ATT ? 2 : 1);
      UNROLL for (int pp = 0; pp < 2; ++pp) { b[pp][0] = lds128(L, L_X + xo + pp * 2048); b[pp][1] = lds128(L, L_X + xo + pp * 2048 + 256); }
      if (ATT) { res[0] = lds128(L, L_WRES + (wr * 14) * 1024 + l16); res[1] = lds128(L, L_WRES + (wr * 14 + 1) * 1024 + l16); }
      SBAR();
      UNROLL for (int pp = 0; pp < 14; ++pp) {
        if (pp + 2 < 14) { b[(pp + 2) % 3][0] = lds128(L, L_X + xo + (pp + 2) * 2048); b[(pp + 2) % 3][1] = lds128(L, L_X + xo + (pp + 2) * 2048 + 256); }
        if (pp == 5) { const int so = L_ST + (lane & 15) * 64 + (lane >> 4) * 16; st[0].a = ldsf4(L, so); st[1].a = ldsf4(L, so + 1024); }
        if (pp == 13) { UNROLL for (int i = 0; i < NT; ++i) { bv[i] = ldsf4(L, L_B1 + FT(i) * 64 + (lane >> 4) * 16); csv[i] = ldsf4(L, L_CS + FT(i) * 64 + (lane >> 4) * 16); } }
        UNROLL for (int i = 0; i < NT; ++i) {
          const int f = pp * NT + i;
          v4u cur;
          if (ATT && f < 14) { cur = res[f % 3]; if (f + 2 < 14) res[(f + 2) % 3] = lds128(L, L_WRES + (wr * 14 + f + 2) * 1024 + l16); }
          else cur = ring[(ATT ? f - 14 : f) % RING];
          mmax(pp == 0, acc[i][0], cur, b[pp % 3][0]); mmax(pp == 0, acc[i][1], cur, b[pp % 3][1]);
          SBAR();
          if (!ATT && (f & 1) == 0) { SLOAD(38 + (f >> 1)); SBAR(); }
          if (ATT && f >= 4 && (f & 1) == 0) { SLOAD(39 + ((f - 4) >> 1)); SBAR(); }
          if (pp >= 7 && pp < 12 && i < 2) { st[i].step(pp - 7); SBAR(); }
          if (pp == 12 && i < 2) { k0r[i] = G0 * st[i].rs; k1r[i] = opq_f(G1 * st[i].rs) * opq_f(st[i].rs * st[i].rs); irs[i] = __builtin_amdgcn_rcpf(st[i].rs); PIN(k0r[i]); PIN(k1r[i]); PIN(irs[i]); SBAR(); }
        }
      }
      STAMP(layer * 32 + 12); FRESH();
      __builtin_amdgcn_s_setprio(0);
      MMA_POST2(acc[NT - 1][0], acc[NT - 1][1]);
      f32x2 K1[2], K0[2], IR[2], NM[2];
      if (!ATT) { UNROLL for (int tt = 0; tt < 2; ++tt) { K0[tt] = dup2(k0r[tt]); K1[tt] = dup2(k1r[tt]); IR[tt] = dup2(irs[tt]); NM[tt] = dup2(-st[tt].mu); } }
      UNROLL for (int h = 0; h < 2; ++h) UNROLL for (int tt = 0; tt < 2; ++tt) {
        v4u y;
        UNROLL for (int e = 0; e < 2; ++e) {
          const int i = 2 * h + e;
          if (i < NT) {
            if (ATT) {
              float z[4];
              UNROLL for (int j = 0; j < 4; ++j) z[j] = gelu_s(__builtin_fmaf(bv[i][j], irs[tt], __builtin_fmaf(-st[tt].mu, csv[i][j], acc[i][tt][j])), k1r[tt], k0r[tt], irs[tt]);
              y[2 * e] = pk2(z[0], z[1]); y[2 * e + 1] = pk2(z[2], z[3]);
            } else {
              y[2 * e] = gelu_p(pkfma(f32x2{bv[i][0], bv[i][1]}, IR[tt], pkfma(f32x2{csv[i][0], csv[i][1]}, NM[tt], f32x2{acc[i][tt][0], acc[i][tt][1]})), K1[tt], K0[tt], IR[tt]);
              y[2 * e + 1] = gelu_p(pkfma(f32x2{bv[i][2], bv[i][3]}, IR[tt], pkfma(f32x2{csv[i][2], csv[i][3]}, NM[tt], f32x2{acc[i][tt][2], acc[i][tt][3]})), K1[tt], K0[tt], IR[tt]);
            }
            PIN(y);
            SBAR();
            if (!ATT) SRANGE(66 + 4 * h + 2 * tt + e, 67 + 4 * h + 2 * tt + e);
          }
        }
        if (ATT && h == 1) *(LAS v2u*)(L + L_H + xb + tt * 256) = v2u{y[0], y[1]}; else *(LAS v4u*)(L + L_H + xa + h * 2048 + tt * 256) = y;
        SBAR();
      }
      STAMP(layer * 32 + 25);
      if (ATT) { UNROLL for (int s = 58; s < 70; ++s) { SLOAD(s); __builtin_amdgcn_s_sleep(1); SBAR(); } }
      STAMP(layer * 32 + 13);
      BARRIER();
      STAMP(layer * 32 + 14); FRESH();
      if (!ATT) __builtin_amdgcn_s_setprio(1);
      UNROLL for (int pp = 0; pp < 2; ++pp) { b[pp][0] = lds128(L, L_H + xo + pp * 2048); b[pp][1] = lds128(L, L_H + xo + pp * 2048 + 256); }
      UNROLL for (int pp = 0; pp < 14; ++pp) {
        if (pp + 2 < 14) { b[(pp + 2) % 3][0] = lds128(L, L_H + xo + (pp + 2) * 2048); b[(pp + 2) % 3][1] = lds128(L, L_H + xo + (pp + 2) * 2048 + 256); }
        UNROLL for (int i = 0; i < NT; ++i) {
          const int f = pp * NT + i;
          const v4u cur = ring[(N1 + f) % RING];
          mmax(pp == 0, acc[i][0], cur, b[pp % 3][0]); mmax(pp == 0, acc[i][1], cur, b[pp % 3][1]);
          SBAR();
          if (!ATT && f < 38) { SLOAD(74 + f); SBAR(); }
        }
      }
      __builtin_amdgcn_s_setprio(0);
      MMA_POST2(acc[NT - 1][0], acc[NT - 1][1]);
    }
    STAMP(layer * 32 + 15); FRESH();
    UNROLL for (int i = 0; i < NT; ++i) if (layer == 0 || (FT(i) & 3) != c) st128(pk8(acc[i][0], acc[i][1]), rs, l16 + i * 1024, pb + (layer * 2 + 1) * PART_BYTES + c * 28672);
    SBAR();
    add_base(true);
    {
      const bool last = arrive_last(L, 0, 8 * (2 * layer + 2), lane);
      STAMP(layer * 32 + 16); FRESH();
      if (last) signal_and_wait(rs, layer * 2 + 1, grp, c, lane, tlo, thi);
    }
    if (layer == 1) break;
    {
      v4u s0, s1;
      make_sel(s0, s1, lane);
      if (ATT && kvrole < 2) {
        v4u wq[14], wf[14];
        UNROLL for (int k = 0; k < 14; ++k) wf[k] = ldrun(rs, l16, WS_WKV + (kvrole * 2 + (wr & 1)) * 14 * 1024, k);
        SBAR();
        BARRIER();
        FRESH();
        gather_update(1, s0, s1);
        __builtin_amdgcn_s_setprio(3);
        f32x4 cs;
        if (kvrole) { const float v = ((LAS float*)(L + L_B2 + 1792 + (wr & 1) * 64))[lane & 15]; cs = f32x4{v, v, v, v}; } else cs = ldsf4(L, L_B2 + 1792 + (wr & 1) * 64 + (lane >> 4) * 16);
        BARRIER();
        if (kvrole) kv_produce<true, false>(wf, cs, L, rs, 1, grp, wr, lane, xo); else kv_produce<false, false>(wf, cs, L, rs, 1, grp, wr, lane, xo);
        STAMP(20);
        kv_signal(L, rs, 1, grp, kvrole, lane, tlo, thi);
        STAMP(21);
        UNROLL for (int k = 0; k < 14; ++k) wq[k] = ldrun(rs, l16, WS_WQ + (8 * c + w) * 14 * 1024, k);
        SBAR();
        cs = ldsf4(L, csq_o);
        q_proj<false>(wq, cs, L, w, lane, xo);
        __builtin_amdgcn_s_setprio(0);
      } else {
        v4u wq[14];
        if (ATT) { UNROLL for (int k = 0; k < 14; ++k) wq[k] = ldrun(rs, l16, WS_WQ + (8 * c + w) * 14 * 1024, k); SBAR(); }
        BARRIER();
        STAMP(layer * 32 + 17); FRESH();
        gather_update(1, s0, s1);
        STAMP(layer * 32 + 18);
        BARRIER();
        STAMP(layer * 32 + 19);
        if (!ATT) { UNROLL for (int k = 0; k < 14; ++k) wq[k] = ldrun(rs, l16, WS_WQ + (8 * c + w) * 14 * 1024, k); SBAR(); }
        f32x4 cs = ldsf4(L, csq_o);
        q_proj<false>(wq, cs, L, w, lane, xo);
      }
    }
    gate(1);
    if (!ATT) { asm volatile("s_waitcnt lgkmcnt(0)" ::: "memory"); if (lane == 0) __hip_atomic_fetch_add((LAS unsigned*)(L + L_MISC + 48), 1u, __ATOMIC_RELAXED, __HIP_MEMORY_SCOPE_WORKGROUP); }
  }
  {
    BARRIER();
    STAMP(49);
    UNROLL for (int i = 0; i < NT; ++i) {
      const int ft = FT(i);
      if ((ft & 3) == c) {
        v4u pr[3];
        UNROLL for (int k = 0; k < 3; ++k) pr[k] = ldf<SC1>(rs, l16 + i * 1024, pb + 3 * PART_BYTES + (c ^ (k + 1)) * 28672);
        UNROLL for (int tt = 0; tt < 2; ++tt) {
          const f32x4 s = (acc[i][tt] + up4(pr[0][2 * tt], pr[0][2 * tt + 1])) + (up4(pr[1][2 * tt], pr[1][2 * tt + 1]) + up4(pr[2][2 * tt], pr[2][2 * tt + 1]));
          *(GAS v2u*)(p.out + (32 * grp + 16 * tt + (lane & 15)) * DM + 16 * ft + 4 * (lane >> 4)) = pk4(s);
        }
      }
    }
    STAMP(50);
  }
  STAMP_FLUSH();
}

__global__ void __launch_bounds__(512, 2) fused_layers(Params p) {
  extern __shared__ __attribute__((aligned(16))) unsigned char smem[];
  LAS unsigned char* L = (LAS unsigned char*)smem;
  const int lane = threadIdx.x & 63, w = __builtin_amdgcn_readfirstlane(threadIdx.x >> 6);
  const int b = blockIdx.x, grp = (b & 7) * 8 + (b >> 5), c = (b >> 3) & 3;
  if (w == 7) {
    const unsigned long long pc = __builtin_amdgcn_s_getpc();
    touch(mkrs4((const void*)(pc & 0xffffffffff80ull), CODEWARM * 1024), lane * 128, ((b >> 3) & 7) * 8192, (unsigned)(size_t)L + L_MISC + 768);
  }
  if (w < 4) wave_main<true>(p, L, grp, c, w, lane); else wave_main<false>(p, L, grp, c, w, lane);
}

extern "C" void kernel_launch(void* const* d_in, const int* in_sizes, int n_in, void* d_out, int out_size, void* d_ws, size_t ws_size, hipStream_t stream) {
  Params p;
  p.x = (const u16*)d_in[0]; p.wq = (const u16*)d_in[1]; p.wk = (const u16*)d_in[2]; p.wv = (const u16*)d_in[3]; p.wo = (const u16*)d_in[4];
  p.w1 = (const u16*)d_in[6]; p.b1 = (const u16*)d_in[7]; p.w2 = (const u16*)d_in[8]; p.b2 = (const u16*)d_in[9];
  p.out = (u16*)d_out; p.ws = (unsigned char*)d_ws;
  (void)hipFuncSetAttribute((const void*)fused_layers, hipFuncAttributeMaxDynamicSharedMemorySize, LDS_BYTES);
  fused_layers<<<256, 512, LDS_BYTES, stream>>>(p);
}
```
